```python
import torch

def _softmax(x, axis=-1):
    return torch.softmax(x, dim=axis)
def _gelu(x):
    return torch.nn.functional.gelu(x, approximate="tanh")
def _silu(x):
    return torch.nn.functional.silu(x)
def _sigmoid(x):
    return torch.sigmoid(x)
def _layernorm(x, axis=-1, eps=1e-5):
    m = x.mean(dim=axis, keepdim=True)
    v = x.var(dim=axis, keepdim=True, unbiased=False)
    return (x - m) / torch.sqrt(v + eps)
def _scatter_add(tgt, idx, src, axis=0):
    return tgt.clone().index_add_(axis, idx, src)
def _segment_sum(src, seg, n):
    out = torch.zeros((n,) + src.shape[1:], dtype=src.dtype)
    return out.index_add_(0, seg, src)
def _pad(x, pw):
    flat = tuple(v for pair in reversed(pw) for v in pair)
    return torch.nn.functional.pad(x, flat)

kernel_name = 'concat_heads__ssm_conv_s5__ssm_conv_s4d__prompt'

def setup_inputs():

    _w = (lambda a: (lambda t: t.to(torch.bfloat16) if t.is_floating_point() else t)(torch.as_tensor(a)))
    def _A_setup():
        B, S, D = 1, 2048, 576
        P = D
        Lambda_re = -0.5 * (lambda _x: torch.exp(torch.as_tensor(_x)))((lambda *s: torch.randn(*s, dtype=torch.bfloat16) * 0.2)(P))
        Lambda_im = (lambda *s: torch.randn(*s, dtype=torch.bfloat16) * 0.4)(P) * 1.0
        B_re = (lambda *s: torch.randn(*s, dtype=torch.bfloat16) * 0.2)(P, D) * (D ** -0.5)
        B_im = (lambda *s: torch.randn(*s, dtype=torch.bfloat16) * 0.2)(P, D) * (D ** -0.5)
        C_re = (lambda *s: torch.randn(*s, dtype=torch.bfloat16) * 0.2)(D, P) * (P ** -0.5)
        C_im = (lambda *s: torch.randn(*s, dtype=torch.bfloat16) * 0.2)(D, P) * (P ** -0.5)
        log_dt = (lambda *s: torch.randn(*s, dtype=torch.bfloat16) * 0.2)(P) * 0.3 - 2.0
        Dskip = (lambda *s: torch.randn(*s, dtype=torch.bfloat16) * 0.05)(D)
        return {
            "x": _w((lambda *s: torch.randn(*s, dtype=torch.bfloat16) * 0.05)(B, S, D)),
            "Lambda_re": _w(Lambda_re),
            "Lambda_im": _w(Lambda_im),
            "B_re": _w(B_re),
            "B_im": _w(B_im),
            "C_re": _w(C_re),
            "C_im": _w(C_im),
            "log_dt": _w(log_dt),
            "Dskip": _w(Dskip),
        }
    _A_in = _A_setup()

    def _B_setup():
        Bsz, L, D, N = 1, 2048, 576, 128
        return {
            "u": _w((lambda *s: torch.randn(*s, dtype=torch.bfloat16) * 0.05)(Bsz, L, D)),
            "log_A_real": _w((lambda *s: torch.randn(*s, dtype=torch.bfloat16) * 3.2)(D, N) * 0.5),
            "A_imag": _w((lambda *s: torch.randn(*s, dtype=torch.bfloat16) * 0.05)(D, N) * 3.14),
            "C_real": _w((lambda *s: torch.randn(*s, dtype=torch.bfloat16) * 0.1)(D, N) / N ** 0.5),
            "C_imag": _w((lambda *s: torch.randn(*s, dtype=torch.bfloat16) * 0.1)(D, N) / N ** 0.5),
            "log_dt": _w((lambda *s: torch.randn(*s, dtype=torch.bfloat16) * 0.05)(D) * 0.5 - 4.0),
            "D_skip": _w((lambda *s: torch.randn(*s, dtype=torch.bfloat16) * 0.05)(D)),
        }
    _B_in = _B_setup()

    return {
        "A_x": _A_in["x"],
        "A_Lambda_re": _A_in["Lambda_re"],
        "A_Lambda_im": _A_in["Lambda_im"],
        "A_B_re": _A_in["B_re"],
        "A_B_im": _A_in["B_im"],
        "A_C_re": _A_in["C_re"],
        "A_C_im": _A_in["C_im"],
        "A_log_dt": _A_in["log_dt"],
        "A_Dskip": _A_in["Dskip"],
        "B_u": _B_in["u"],
        "B_log_A_real": _B_in["log_A_real"],
        "B_A_imag": _B_in["A_imag"],
        "B_C_real": _B_in["C_real"],
        "B_C_imag": _B_in["C_imag"],
        "B_log_dt": _B_in["log_dt"],
        "B_D_skip": _B_in["D_skip"],
    }

def reference(A_x, A_Lambda_re, A_Lambda_im, A_B_re, A_B_im, A_C_re, A_C_im, A_log_dt, A_Dskip, B_u, B_log_A_real, B_A_imag, B_C_real, B_C_imag, B_log_dt, B_D_skip):
    def _A_ref(x, Lambda_re, Lambda_im, B_re, B_im, C_re, C_im, log_dt, Dskip):
        Bsz, L, H = x.shape
        Lam = Lambda_re + 1j * Lambda_im
        Bmat = B_re + 1j * B_im
        Cmat = C_re + 1j * C_im
        dt = torch.exp(log_dt)
        Lam_bar = torch.exp(dt * Lam)
        B_bar = ((Lam_bar - 1.0) / Lam)[:, None] * Bmat
        Bu = torch.einsum("bld,pd->blp", x + 0j, B_bar)
        h = torch.zeros_like(Bu[:, 0, :])
        hs = []
        for t in range(L):
            h = Lam_bar[None, :] * h + Bu[:, t, :]
            hs.append(h)
        hseq = torch.stack(hs, 1)
        y = 2.0 * torch.einsum("blp,dp->bld", hseq, Cmat).real + x * Dskip[None, None, :]
        return y

    def _B_ref(u, log_A_real, A_imag, C_real, C_imag, log_dt, D_skip):
        L = u.shape[1]
        dt = torch.exp(log_dt)
        A = -torch.exp(log_A_real) + 1j * A_imag
        C = (C_real + 1j * C_imag) * dt[:, None]
        pos = (lambda x, dt: x.to(dt))(torch.arange(L), log_dt.dtype)
        P = torch.exp((A * dt[:, None])[:, :, None] * pos[None, None, :])
        K = 2.0 * torch.einsum("dn,dnl->dl", C, P).real
        L2 = 2 * L
        u_t = (lambda x, *d: x.permute(*d))(u, 0, 2, 1)
        Kf = (lambda x, *a, **k: torch.fft.rfft(x.float(), *a, **k))(K, L2)
        Uf = (lambda x, *a, **k: torch.fft.rfft(x.float(), *a, **k))(u_t, L2)
        y = (lambda x, *a, **k: torch.fft.irfft(x, *a, **k).to(torch.bfloat16))(Uf * Kf[None, :, :], L2)[:, :, :L]
        y = (lambda x, *d: x.permute(*d))(y, 0, 2, 1)
        return y + u * D_skip[None, None, :]

    a = _A_ref(A_x, A_Lambda_re, A_Lambda_im, A_B_re, A_B_im, A_C_re, A_C_im, A_log_dt, A_Dskip)
    b = _B_ref(B_u, B_log_A_real, B_A_imag, B_C_real, B_C_imag, B_log_dt, B_D_skip)
    return torch.concatenate([a, b], axis=-1)
```

```cpp
#include <hip/hip_runtime.h>
#include <hip/hip_bf16.h>
#include <stdint.h>

#define L_ 2048
#define D_ 576
#define P_ 576
#define N_ 128

typedef unsigned short u16;
__device__ __forceinline__ float bf2f(u16 v){ return __uint_as_float(((unsigned)v)<<16); }
__device__ __forceinline__ float rbf(float f){
  unsigned u=__float_as_uint(f); u = (u + 0x7FFFu + ((u>>16)&1u)) & 0xFFFF0000u; return __uint_as_float(u); }

#define ROUND_H 0
#define ROUND_K 0

__global__ void k_gemm1(const u16* x,const u16* Bre,const u16* Bim,const u16* Lre,const u16* Lim,const u16* ldt,float2* Bu){
  int p = blockIdx.x*64+threadIdx.x; int t = blockIdx.y;
  float sr=0,si=0;
  for(int d=0;d<D_;++d){ float xv=bf2f(x[t*D_+d]); sr+=xv*bf2f(Bre[p*D_+d]); si+=xv*bf2f(Bim[p*D_+d]); }
  float lr=bf2f(Lre[p]), li=bf2f(Lim[p]); float dt=rbf(expf(bf2f(ldt[p])));
  float e=expf(dt*lr); float br=e*cosf(dt*li), bi=e*sinf(dt*li);
  float nr=br-1.f, ni=bi; float den=lr*lr+li*li; float cr=(nr*lr+ni*li)/den, ci=(ni*lr-nr*li)/den;
  Bu[t*P_+p]=make_float2(cr*sr-ci*si, cr*si+ci*sr);
}
__global__ void k_scan(float2* Bu,const u16* Lre,const u16* Lim,const u16* ldt){
  int p = blockIdx.x*64+threadIdx.x;
  float lr=bf2f(Lre[p]), li=bf2f(Lim[p]); float dt=rbf(expf(bf2f(ldt[p])));
  float e=expf(dt*lr); float br=e*cosf(dt*li), bi=e*sinf(dt*li);
  float hr=0,hi=0;
  for(int t=0;t<L_;++t){ float2 b=Bu[t*P_+p]; float nr=br*hr-bi*hi+b.x, ni=br*hi+bi*hr+b.y; hr=nr;hi=ni;
    Bu[t*P_+p]= ROUND_H? make_float2(rbf(hr),rbf(hi)) : make_float2(hr,hi); }
}
__global__ void k_gemm2(const float2* h,const u16* Cre,const u16* Cim,const u16* x,const u16* Dsk,float* out){
  int d = blockIdx.x*64+threadIdx.x; int t = blockIdx.y;
  float s=0;
  for(int p=0;p<P_;++p){ float2 hv=h[t*P_+p]; s+=hv.x*bf2f(Cre[d*P_+p])-hv.y*bf2f(Cim[d*P_+p]); }
  out[t*(2*D_)+d]=2.f*s+rbf(bf2f(x[t*D_+d])*bf2f(Dsk[d]));
}
__global__ void k_kgen(const u16* lA,const u16* Aim,const u16* Cr,const u16* Ci,const u16* ldt,float* K){
  int l = blockIdx.x*64+threadIdx.x; int d = blockIdx.y;
  float dt=rbf(expf(bf2f(ldt[d]))); float pos=rbf((float)l);
  float s=0;
  for(int n=0;n<N_;++n){
    float ar=-rbf(expf(bf2f(lA[d*N_+n])))*dt, ai=bf2f(Aim[d*N_+n])*dt;
    float cr=bf2f(Cr[d*N_+n])*dt, ci=bf2f(Ci[d*N_+n])*dt;
    float e=expf(ar*pos); float sn,cs; sincosf(ai*pos,&sn,&cs);
    s+=e*(cr*cs-ci*sn);
  }
  K[d*L_+l]= ROUND_K? rbf(2.f*s) : 2.f*s;
}
__global__ void k_conv(const float* K,const u16* u,const u16* Dsk,float* out){
  int d = blockIdx.x*64+threadIdx.x; int t = blockIdx.y;
  float s=0;
  for(int j=0;j<=t;++j) s+=K[d*L_+j]*bf2f(u[(t-j)*D_+d]);
  out[t*(2*D_)+D_+d]=rbf(rbf(s)+rbf(bf2f(u[t*D_+d])*bf2f(Dsk[d])));
}

extern "C" void kernel_launch(void* const* d_in, const int* in_sizes, int n_in,
                              void* d_out, int out_size, void* d_ws, size_t ws_size,
                              hipStream_t stream) {
  const u16* x=(const u16*)d_in[0]; const u16* Lre=(const u16*)d_in[1]; const u16* Lim=(const u16*)d_in[2];
  const u16* Bre=(const u16*)d_in[3]; const u16* Bim=(const u16*)d_in[4]; const u16* Cre=(const u16*)d_in[5]; const u16* Cim=(const u16*)d_in[6];
  const u16* ldtA=(const u16*)d_in[7]; const u16* DA=(const u16*)d_in[8];
  const u16* u=(const u16*)d_in[9]; const u16* lA=(const u16*)d_in[10]; const u16* Aim=(const u16*)d_in[11];
  const u16* Cr=(const u16*)d_in[12]; const u16* Ci=(const u16*)d_in[13]; const u16* ldtB=(const u16*)d_in[14]; const u16* DB=(const u16*)d_in[15];
  float* out=(float*)d_out;
  float2* Bu=(float2*)d_ws; float* K=(float*)((char*)d_ws+ (size_t)L_*P_*8);
  k_gemm1<<<dim3(P_/64,L_),64,0,stream>>>(x,Bre,Bim,Lre,Lim,ldtA,Bu);
  k_scan<<<P_/64,64,0,stream>>>(Bu,Lre,Lim,ldtA);
  k_gemm2<<<dim3(D_/64,L_),64,0,stream>>>(Bu,Cre,Cim,x,DA,out);
  k_kgen<<<dim3(L_/64,D_),64,0,stream>>>(lA,Aim,Cr,Ci,ldtB,K);
  k_conv<<<dim3(D_/64,L_),64,0,stream>>>(K,u,DB,out);
}
```

```cpp
#define STAMP_ON 1
#define AMP_K 0
#define AMP_ROLE 0
#define AMP_F 0
#define AMP_W 0
#include <hip/hip_runtime.h>
#include <stdint.h>

typedef unsigned short u16;
typedef unsigned long long u64;
typedef unsigned v4u __attribute__((ext_vector_type(4)));
typedef unsigned v2u __attribute__((ext_vector_type(2)));
typedef float f32x4 __attribute__((ext_vector_type(4)));
typedef float f32x2 __attribute__((ext_vector_type(2)));
typedef __bf16 bf16x8 __attribute__((ext_vector_type(8)));
typedef __bf16 bf16x2 __attribute__((ext_vector_type(2)));
#define CAS __attribute__((address_space(4)))
#define LAS __attribute__((address_space(3)))
#define GAS __attribute__((address_space(1)))
#define DEV __device__ __forceinline__

constexpr int WS_FLAG = 4096;
constexpr int WS_E = 65536;
constexpr int WS_H = 262144;
constexpr int WS_END = WS_H + 2048 * 1152 * 2;
constexpr int WS_DBG = 8 << 20;
constexpr unsigned SPIN_CAP = 400000u;
constexpr u64 TAG_STEP = 0x9E3779B97F4A7C15ull;
#define CODE_LINES 170

#define ROTATE 0
#define PREFETCH 1
#define STAMP(i) do { __builtin_amdgcn_sched_barrier(0); asm volatile("s_memrealtime %0\n\ts_waitcnt lgkmcnt(0)" : "=s"(stamp[i]) :: "memory"); __builtin_amdgcn_sched_barrier(0); } while (0)
#define STAMP_FLUSH() do { if (lane == 0 && w == AMP_W) { GAS u64* sp = (GAS u64*)(ws + WS_DBG) + b * 16; const u64 tg_ = *(const CAS u64*)ws + TAG_STEP; \
    __hip_atomic_store(sp + 0, stamp[0], __ATOMIC_RELAXED, __HIP_MEMORY_SCOPE_AGENT); __hip_atomic_store(sp + 1, stamp[AMP_K], __ATOMIC_RELAXED, __HIP_MEMORY_SCOPE_AGENT); \
    WAITV(0); __hip_atomic_store(sp + 2, tg_, __ATOMIC_RELAXED, __HIP_MEMORY_SCOPE_AGENT); } } while (0)

#define WAITV(n) asm volatile("s_waitcnt vmcnt(%0)" ::"n"(n) : "memory")
#define BARRIER() asm volatile("s_waitcnt lgkmcnt(0)\n\ts_barrier" ::: "memory")
#define FENCE() asm volatile("" ::: "memory")
#define SCHED() __builtin_amdgcn_sched_barrier(0)

struct c2 { f32x2 re, im; };
DEV f32x2 fma2(f32x2 a, f32x2 b, f32x2 c) { return __builtin_elementwise_fma(a, b, c); }
DEV f32x2 sp(float a) { return f32x2{a, a}; }
DEV c2 cmul(c2 a, c2 b) { return {fma2(a.re, b.re, -(a.im * b.im)), fma2(a.re, b.im, a.im * b.re)}; }
DEV c2 cfma(c2 a, c2 b, c2 c) { return {fma2(a.re, b.re, fma2(-a.im, b.im, c.re)), fma2(a.re, b.im, fma2(a.im, b.re, c.im))}; }
DEV c2 csqr(c2 a) { return {fma2(a.re, a.re, -(a.im * a.im)), (a.re + a.re) * a.im}; }
DEV c2 el(c2 a, int t) { return {sp(a.re[t]), sp(a.im[t])}; }
DEV float bflo(unsigned v) { return __uint_as_float(v << 16); }
DEV float bfhi(unsigned v) { return __uint_as_float(v & 0xFFFF0000u); }
DEV f32x2 bf2(unsigned v) { return f32x2{bflo(v), bfhi(v)}; }
DEV unsigned pk(float lo, float hi) { f32x2 v = {lo, hi}; return __builtin_bit_cast(unsigned, __builtin_convertvector(v, bf16x2)); }
DEV unsigned pk2(f32x2 v) { return __builtin_bit_cast(unsigned, __builtin_convertvector(v, bf16x2)); }
DEV float rbf(float f) { return bflo(pk(f, f)); }
constexpr float LOG2E = 1.44269504088896341f, INV2PI = 0.159154943091895336f;
DEV f32x2 exp2v(f32x2 x) { return f32x2{__builtin_amdgcn_exp2f(x[0]), __builtin_amdgcn_exp2f(x[1])}; }
DEV c2 cpow(f32x2 l2, f32x2 rev, f32x2 n) {
  const f32x2 m = exp2v(l2 * n), t0 = rev * n;
  const float ta = __builtin_amdgcn_fractf(t0[0]), tb = __builtin_amdgcn_fractf(t0[1]);
  return {m * f32x2{__builtin_amdgcn_cosf(ta), __builtin_amdgcn_cosf(tb)}, m * f32x2{__builtin_amdgcn_sinf(ta), __builtin_amdgcn_sinf(tb)}};
}
DEV f32x2 redrev(f32x2 ai) { const f32x2 r = ai * INV2PI; return r - f32x2{__builtin_rintf(r[0]), __builtin_rintf(r[1])}; }
DEV f32x4 mfma(v4u a, v4u b, f32x4 c) {
  return __builtin_amdgcn_mfma_f32_16x16x32_bf16(__builtin_bit_cast(bf16x8, a), __builtin_bit_cast(bf16x8, b), c, 0, 0, 0);
}
DEV v4u lds128(LAS char* p) { return *(LAS v4u*)p; }
DEV v2u lds64(LAS char* p) { return *(LAS v2u*)p; }
DEV unsigned perm(unsigned hi, unsigned lo, unsigned sel) { return __builtin_amdgcn_perm(hi, lo, sel); }
DEV f32x2 swap16(f32x2 v, f32x2& hi) {
  auto a = __builtin_amdgcn_permlane16_swap(__float_as_uint(v[0]), __float_as_uint(v[0]), false, false);
  auto b = __builtin_amdgcn_permlane16_swap(__float_as_uint(v[1]), __float_as_uint(v[1]), false, false);
  hi = f32x2{__uint_as_float(a[1]), __uint_as_float(b[1])};
  return f32x2{__uint_as_float(a[0]), __uint_as_float(b[0])};
}
DEV f32x2 swap32(f32x2 v, f32x2& hi) {
  auto a = __builtin_amdgcn_permlane32_swap(__float_as_uint(v[0]), __float_as_uint(v[0]), false, false);
  auto b = __builtin_amdgcn_permlane32_swap(__float_as_uint(v[1]), __float_as_uint(v[1]), false, false);
  hi = f32x2{__uint_as_float(a[1]), __uint_as_float(b[1])};
  return f32x2{__uint_as_float(a[0]), __uint_as_float(b[0])};
}
DEV v4u quadT(unsigned x0, unsigned x1, unsigned x2, unsigned x3, bool p, bool q) {
  const unsigned a0 = __builtin_amdgcn_mov_dpp(x0, 0xB1, 0xF, 0xF, true), a1 = __builtin_amdgcn_mov_dpp(x1, 0xB1, 0xF, 0xF, true);
  const unsigned a2 = __builtin_amdgcn_mov_dpp(x2, 0xB1, 0xF, 0xF, true), a3 = __builtin_amdgcn_mov_dpp(x3, 0xB1, 0xF, 0xF, true);
  const unsigned y0 = p ? a1 : x0, y1 = p ? x1 : a0, y2 = p ? a3 : x2, y3 = p ? x3 : a2;
  const unsigned d0 = __builtin_amdgcn_mov_dpp(y0, 0x4E, 0xF, 0xF, true), d1 = __builtin_amdgcn_mov_dpp(y1, 0x4E, 0xF, 0xF, true);
  const unsigned d2 = __builtin_amdgcn_mov_dpp(y2, 0x4E, 0xF, 0xF, true), d3 = __builtin_amdgcn_mov_dpp(y3, 0x4E, 0xF, 0xF, true);
  return v4u{q ? d2 : y0, q ? d3 : y1, q ? y2 : d0, q ? y3 : d1};
}
DEV c2 cj(c2 a) { return {a.re, -a.im}; }
DEV c2 cmulj(c2 a, c2 b) { return {fma2(a.re, b.re, -(a.im * b.im)), fma2(-a.re, b.im, -(a.im * b.re))}; }

__global__ __launch_bounds__(512, 2) void fused_ssm(
    const u16* __restrict__ x, const u16* __restrict__ Lre, const u16* __restrict__ Lim, const u16* __restrict__ Bre,
    const u16* __restrict__ Bim, const u16* __restrict__ Cre, const u16* __restrict__ Cim, const u16* __restrict__ ldtA,
    const u16* __restrict__ DA, const u16* __restrict__ u, const u16* __restrict__ lA, const u16* __restrict__ Aim,
    const u16* __restrict__ Cr, const u16* __restrict__ Ci, const u16* __restrict__ ldtB, const u16* __restrict__ DB,
    float* out, char* ws) {
#if defined(__HIP_DEVICE_COMPILE__)
  __shared__ __attribute__((aligned(16))) char smem_[163840];
  LAS char* smem = (LAS char*)smem_;
  u64 stamp[16] = {};
  u64 pc0;
  asm volatile("s_getpc_b64 %0" : "=s"(pc0));
  const int b = blockIdx.x, xcd = b & 7, j = b >> 3;
  const int tid = threadIdx.x, lane = tid & 63, w = __builtin_amdgcn_readfirstlane(tid >> 6);
  const int r = lane & 15, g = lane >> 4;
  STAMP(0);

  if (__builtin_expect(j >= 18, 0)) {
  if (j < 27) {
    const int d0 = 72 * xcd + 8 * (j - 18), d = d0 + w;
    LAS char* sc = smem + w * 20464;
    const int tb = ((tid >> 1) + 32 * (j - 18)) & 255, hf = tid & 1;
    const unsigned qa = *(const GAS unsigned*)(lA + d * 128 + 2 * lane), qi = *(const GAS unsigned*)(Aim + d * 128 + 2 * lane);
    const unsigned qr = *(const GAS unsigned*)(Cr + d * 128 + 2 * lane), qc = *(const GAS unsigned*)(Ci + d * 128 + 2 * lane);
    const v2u dk = *(const GAS v2u*)(DB + d0 + 4 * hf);
    const float dt = rbf(__builtin_amdgcn_exp2f(bflo(ldtB[d]) * LOG2E));
    const f32x2 l2 = bf2(pk2(exp2v(bf2(qa) * LOG2E))) * (-dt) * LOG2E, rv = redrev(bf2(qi) * dt);
    FENCE();
    STAMP(1);
    v2u uv[8];
    const auto rsU = __builtin_amdgcn_make_buffer_rsrc((void*)u, 0, 2048 * 1152, 0x00020000);
    const unsigned uvo = (8 * tb) * 1152 + (d0 + 4 * hf) * 2;
#define ULOAD(i) do { FENCE(); uv[i] = __builtin_amdgcn_raw_buffer_load_b64(rsU, uvo, (i) * 1152, 0); FENCE(); } while (0)
    ULOAD(0); ULOAD(1);
    f32x4 kacc[3] = {};
    {
      const c2 w1 = cpow(l2, rv, sp(1.f)), v8 = cpow(l2, -rv, sp(8.f)), Q = cpow(l2, rv, sp(128.f));
      LAS char* tw = sc + lane * 8;
      LAS char* tr = sc + r * 528 + g * 16;
#define PUTG(row, z) (*(LAS v2u*)(tw + (row) * 528) = v2u{pk(z.re[0], z.im[0]), pk(z.re[1], z.im[1])})
      *(LAS v2u*)(tw) = v2u{0x3F80u, 0x3F80u};
      c2 f = v8;
      PUTG(1, f);
#pragma unroll
      for (int i = 2; i < 16; ++i) { f = cmul(f, v8); PUTG(i, f); if (i == 4) ULOAD(2); if (i == 8) ULOAD(3); if (i == 12) ULOAD(4); }
      ULOAD(5);
      v4u fF[8];
#pragma unroll
      for (int ks = 0; ks < 8; ++ks) fF[ks] = lds128(tr + ks * 64);
      c2 R[8];
      R[0] = {bf2(qr) * (2.f * dt), bf2(qc) * (2.f * dt)};
#pragma unroll
      for (int i = 1; i < 8; ++i) R[i] = cmul(R[i - 1], w1);
      ULOAD(6);
#pragma unroll
      for (int i = 0; i < 8; ++i) { PUTG(i, R[i]); R[i] = cmul(R[i], Q); PUTG(8 + i, R[i]); if (i == 2) ULOAD(7); }
#pragma unroll
      for (int ks = 0; ks < 8; ++ks) kacc[0] = mfma(lds128(tr + ks * 64), fF[ks], kacc[0]);
#pragma unroll
      for (int cc = 0; cc < 4; ++cc) {
        const unsigned sel = (cc & 1) ? 0x07060302u : 0x05040100u;
        v4u o;
#pragma unroll
        for (int k = 0; k < 4; ++k) o[k] = perm(uv[2 * k + 1][cc >> 1], uv[2 * k][cc >> 1], sel);
        LAS char* ub = smem + (4 * hf + cc) * 20464 + 8448;
        *(LAS v4u*)(ub + 4640 + (8 * tb + 224) * 2) = o;
        LAS unsigned* u0 = (LAS unsigned*)(ub + (8 * tb + 226) * 2);
        u0[0] = o[0]; u0[1] = o[1]; u0[2] = o[2]; u0[3] = o[3];
      }
      STAMP(2);
      BARRIER();
      if (w < 4) __builtin_amdgcn_s_setprio(1);
#pragma unroll
      for (int s = 0; s < 2; ++s)
#pragma unroll
        for (int i = 0; i < 4; ++i) { R[2 * i] = cmul(R[2 * i], Q); PUTG(4 * s + i, R[2 * i]); }
#pragma unroll
      for (int s = 0; s < 4; ++s)
#pragma unroll
        for (int i = 0; i < 2; ++i) { R[4 * i] = cmul(R[4 * i], Q); PUTG(8 + 2 * s + i, R[4 * i]); }
#pragma unroll
      for (int ks = 0; ks < 8; ++ks) kacc[1] = mfma(lds128(tr + ks * 64), fF[ks], kacc[1]);
#pragma unroll
      for (int s = 0; s < 9; ++s) { R[0] = cmul(R[0], Q); PUTG(s, R[0]); }
#pragma unroll
      for (int ks = 0; ks < 8; ++ks) kacc[2] = mfma(lds128(tr + ks * 64), fF[ks], kacc[2]);
    }
    FENCE();
    STAMP(3);
    {
      LAS char* tt = sc + 17728;
      const unsigned aA = g < 2 ? (4 * r + 64 * g) * 2 : (128 + 2 * r + 64 * (g - 2)) * 2;
      *(LAS unsigned*)(tt + aA) = pk(kacc[1][0], kacc[1][1]);
      *(LAS unsigned*)(tt + aA + (g < 2 ? 4 : 64)) = pk(kacc[1][2], kacc[1][3]);
      const unsigned k01 = pk(kacc[2][0], kacc[2][1]), k23 = pk(kacc[2][2], kacc[2][3]);
      LAS char* t3 = tt + (256 + 64 * g + r) * 2;
      *(LAS u16*)(t3) = (u16)k01; *(LAS u16*)(t3 + 32) = (u16)(k01 >> 16);
      *(LAS u16*)(t3 + 64) = (u16)k23; *(LAS u16*)(t3 + 96) = (u16)(k23 >> 16);
      if (lane < 17) *(LAS unsigned*)(sc + (1023 + lane) * 4) = 0u;
      if (lane < 16) *(LAS unsigned*)(sc + 4224 + (1024 + lane) * 4) = 0u;
      if (lane < 28) { *(LAS v4u*)(sc + 8448 + lane * 16) = v4u{0, 0, 0, 0}; *(LAS v4u*)(sc + 8448 + 4640 + lane * 16) = v4u{0, 0, 0, 0}; }
      if (lane == 28) *(LAS unsigned*)(sc + 8448 + 448) = 0u;
      FENCE();
      const unsigned a1 = *(LAS unsigned*)(tt + lane * 4), b1 = *(LAS unsigned*)(tt + lane * 4 + 4);
      const unsigned a2 = *(LAS unsigned*)(tt + 256 + lane * 4), b2 = *(LAS unsigned*)(tt + 256 + lane * 4 + 4);
      const unsigned a3 = *(LAS unsigned*)(tt + 512 + lane * 4), b3 = *(LAS unsigned*)(tt + 512 + lane * 4 + 4);
      {
        const unsigned e0 = perm(a1, a1, 0x01000100u), m10 = perm(a1, a1, 0x01000302u), m21 = perm(a1, b1, 0x07060100u);
        *(LAS v2u*)(sc + 4224 + (1788 - 4 * lane) * 2) = v2u{m21, e0};
        *(LAS v2u*)(sc + (1788 - 4 * lane) * 2) = v2u{m10, e0};
      }
      {
        const unsigned e0 = perm(a2, a2, 0x01000100u), e1 = perm(a2, a2, 0x03020302u), e2 = perm(b2, b2, 0x01000100u);
        const unsigned m10 = perm(a2, a2, 0x01000302u), m21 = perm(a2, b2, 0x07060100u);
        *(LAS v4u*)(sc + 4224 + (1528 - 8 * lane) * 2) = v4u{e2, e1, m10, e0};
        *(LAS v4u*)(sc + (1528 - 8 * lane) * 2) = v4u{m21, e1, e0, e0};
      }
      {
        const unsigned e0 = perm(a3, a3, 0x01000100u), e1 = perm(a3, a3, 0x03020302u), e2 = perm(b3, b3, 0x01000100u);
        const unsigned m10 = perm(a3, a3, 0x01000302u), m21 = perm(a3, b3, 0x07060100u);
        LAS char* p1 = sc + 4224 + (1008 - 16 * lane) * 2;
        LAS char* p0_ = sc + (1008 - 16 * lane) * 2;
        *(LAS v4u*)(p1) = v4u{e2, e2, e1, e1}; *(LAS v4u*)(p1 + 16) = v4u{e1, m10, e0, e0};
        *(LAS v4u*)(p0_) = v4u{e2, m21, e1, e1}; *(LAS v4u*)(p0_ + 16) = v4u{e1, e0, e0, e0};
      }
      FENCE();
      {
        const int q0 = 8 * r + 4 * (g & 1) + 128 * (g >> 1);
        const unsigned v32 = pk(kacc[0][3], kacc[0][2]), v10 = pk(kacc[0][1], kacc[0][0]), v21 = pk(kacc[0][2], kacc[0][1]);
        *(LAS v2u*)(sc + 4224 + (2044 - q0) * 2) = v2u{v32, v10};
        *(LAS u16*)(sc + (2043 - q0) * 2) = (u16)v32;
        *(LAS unsigned*)(sc + (2044 - q0) * 2) = v21;
        *(LAS u16*)(sc + (2046 - q0) * 2) = (u16)(v10 >> 16);
      }
    }
    FENCE();
    STAMP(4);
    {
      f32x4 ya[8] = {};
      LAS char* pT = sc + (r & 1) * 4224 + 504 - 8 * (r >> 1) + 16 * g;
      LAS char* pT2 = pT + 8;
      asm volatile("" : "+v"(pT2));
      LAS char* pU = sc + 8448 + (r & 1) * 4640 + 448 + 64 * (r >> 1) + 16 * g;
      v4u U[8], Tq[2];
#define LDT(off) ({ const v2u a_ = lds64(pT + (off)), b_ = lds64(pT2 + (off)); v4u{a_[0], a_[1], b_[0], b_[1]}; })
      Tq[0] = LDT(512 * 7);
#pragma unroll
      for (int k = 7; k >= 0; --k) U[k] = lds128(pU + 512 * k);
      Tq[1] = LDT(512 * 6);
#pragma unroll 1
      for (int rr = 0; rr < 8; ++rr) {
#pragma unroll
        for (int m = 0; m < 8; ++m) {
          const v4u T = Tq[m & 1];
          Tq[m & 1] = m < 6 ? LDT(512 * (5 - m)) : LDT(512 * (13 - m) - 64);
#pragma unroll
          for (int k = 7 - m; k >= 0; --k) ya[k + m] = mfma(T, U[k], ya[k + m]);
          U[7 - m] = lds128(pU + 512 * (7 - m) - 64);
          SCHED();
        }
        pT -= 64; pT2 -= 64; pU -= 64;
      }
      FENCE();
      LAS char* py = sc + (4 * g + (r & 1) + 16 * (r >> 1)) * 4;
#pragma unroll
      for (int q = 0; q < 8; ++q) {
        *(LAS unsigned*)(py + 512 * q) = pk(ya[q][0], ya[q][1]);
        *(LAS unsigned*)(py + 512 * q + 8) = pk(ya[q][2], ya[q][3]);
      }
    }
    STAMP(5);
    BARRIER();
    {
      v4u yv[4];
#pragma unroll
      for (int cc = 0; cc < 4; ++cc) yv[cc] = lds128(smem + (4 * hf + cc) * 20464 + 16 * tb);
      float* op = out + (8 * tb) * 1152 + 576 + d0 + 4 * hf;
      const f32x2 d01 = bf2(dk[0]), d23 = bf2(dk[1]);
#pragma unroll
      for (int i = 0; i < 8; ++i) {
        const f32x2 s01 = bf2(pk2(bf2(uv[i][0]) * d01)), s23 = bf2(pk2(bf2(uv[i][1]) * d23));
        f32x2 y01, y23;
        if (i & 1) { y01 = f32x2{bfhi(yv[0][i >> 1]), bfhi(yv[1][i >> 1])}; y23 = f32x2{bfhi(yv[2][i >> 1]), bfhi(yv[3][i >> 1])}; }
        else { y01 = f32x2{bflo(yv[0][i >> 1]), bflo(yv[1][i >> 1])}; y23 = f32x2{bflo(yv[2][i >> 1]), bflo(yv[3][i >> 1])}; }
        const f32x2 o01 = bf2(pk2(y01 + s01)), o23 = bf2(pk2(y23 + s23));
        *(GAS f32x4*)(op + i * 1152) = f32x4{o01[0], o01[1], o23[0], o23[1]};
      }
    }
    STAMP(6);
    STAMP(15);
    STAMP_FLUSH();
  } else if (b != 255) {
    unsigned v = 0;
    if (j == 27 && tid < CODE_LINES) v = *(const GAS unsigned*)((pc0 & ~127ull) + tid * 128);
    if (j < 31) {
      const int R = ((j - 27) * 8 + w) * 36 + (lane < 36 ? lane : 35);
      const u64 bx = (u64)x + (xcd >> 1) * 512 * 1152, br = (u64)Bre + ((xcd & 1) * 256 - 512) * 1152, bi = (u64)Bim + ((xcd & 1) * 256 - 832) * 1152;
      const u64 base = (R < 512 ? bx : (R < 832 ? br : bi)) + R * 1152;
      unsigned t[9];
#pragma unroll
      for (int k = 0; k < 9; ++k) t[k] = *(const GAS unsigned*)(base + k * 128);
#pragma unroll
      for (int k = 0; k < 9; ++k) v ^= t[k];
    }
    asm volatile("" ::"v"(v));
  } else if (w == 0) {
    const u64 tag = *(const CAS u64*)ws + TAG_STEP;
    const GAS u64* fl = (const GAS u64*)(ws + WS_FLAG);
#pragma unroll 1
    for (unsigned sp_ = 0; sp_ < SPIN_CAP; ++sp_) {
      __builtin_amdgcn_s_sleep(16);
      FENCE();
      const u64 f0 = __hip_atomic_load(fl + lane * 16, __ATOMIC_RELAXED, __HIP_MEMORY_SCOPE_AGENT);
      const u64 f1 = __hip_atomic_load(fl + (64 + lane) * 16, __ATOMIC_RELAXED, __HIP_MEMORY_SCOPE_AGENT);
      const u64 f2 = __hip_atomic_load(fl + (128 + (lane & 15)) * 16, __ATOMIC_RELAXED, __HIP_MEMORY_SCOPE_AGENT);
      if (__all(f0 == tag && f1 == tag && f2 == tag)) break;
    }
    {
      u64 mn = ~0ull, mx = 0;
      for (int i = 0; i < 4; ++i) {
        const int bb = i * 64 + lane;
        if (bb < 216) {
          const GAS u64* sp = (const GAS u64*)(ws + WS_DBG) + bb * 16;
          for (unsigned sp_ = 0; sp_ < SPIN_CAP; ++sp_) { if (__hip_atomic_load(sp + 2, __ATOMIC_RELAXED, __HIP_MEMORY_SCOPE_AGENT) == tag) break; __builtin_amdgcn_s_sleep(8); }
          const u64 a = __hip_atomic_load(sp + 0, __ATOMIC_RELAXED, __HIP_MEMORY_SCOPE_AGENT), e = __hip_atomic_load(sp + 1, __ATOMIC_RELAXED, __HIP_MEMORY_SCOPE_AGENT);
          mn = a < mn ? a : mn;
          if ((bb < 144) == (AMP_ROLE == 0)) mx = e > mx ? e : mx;
        }
      }
      for (int o = 1; o < 64; o <<= 1) { const u64 a = __shfl_xor(mn, o), e = __shfl_xor(mx, o); mn = a < mn ? a : mn; mx = e > mx ? e : mx; }
      u64 v = mx - mn; v = v > 10000 ? 10000 : v;
      const u64 until = __builtin_amdgcn_s_memrealtime() + AMP_F * v;
      while (__builtin_amdgcn_s_memrealtime() < until) __builtin_amdgcn_s_sleep(4);
    }
    if (lane == 0) __hip_atomic_store((GAS u64*)ws, tag, __ATOMIC_RELAXED, __HIP_MEMORY_SCOPE_AGENT);
  }
  return;
  }
  {
    const int odd = xcd & 1, mid = j >> 4;
    const int pi = mid ? (j - 16) + 2 * odd : (j >> 2), gi = mid ? 4 : (j & 3);
    const int pg = mid ? 4 : gi + 5 * odd, c = 4 * (xcd >> 1) + pi;
    const int wm = w & 3, wn = w >> 2;
    const int p0 = 64 * pg + 32 * wn + 2 * r;
    const unsigned pLr = *(const GAS unsigned*)(Lre + p0), pLi = *(const GAS unsigned*)(Lim + p0), pDt = *(const GAS unsigned*)(ldtA + p0);
    FENCE();
    const u64 sx = (u64)x + (c * 128 + w * 32) * 1152, sb = (u64)(w < 6 ? Bre : Bim) + (pg * 64 + (w & 1) * 32) * 1152;
    const auto rsS = __builtin_amdgcn_make_buffer_rsrc((void*)(wn ? sb : sx), 0, 32 * 1152, 0x00020000);
    const int l3 = lane >> 3, ch = (lane & 7) ^ (l3 >> 1);
    const int rowx = 8 * (l3 >> 2) + 4 * (l3 & 1) + ((l3 >> 1) & 1), rowb = rowx ^ ((rowx ^ (2 * l3)) & -wn);
    const unsigned dvA = __umul24(rowb, 1152u) + ch * 16, dvB = dvA + 16 * 1152 + 64 - 2 * (dvA & 64);
    const int stp = (2 - wn) * 1152;
#define SLOT(s) (((8 - (s)) % 3) * 32768)
#define SPIECE(s, i) __builtin_amdgcn_raw_ptr_buffer_load_lds(rsS, smem + SLOT(s) + w * 4096 + (i) * 1024, 16, ((i) & 1) ? dvB : dvA, (s) * 128 + ((i) >> 1) * stp, 0, 0)
    SPIECE(0, 0); SPIECE(0, 1); SPIECE(0, 2); SPIECE(0, 3);
    asm volatile("s_barrier" ::: "memory");
    SPIECE(1, 0); SPIECE(1, 1); SPIECE(1, 2); SPIECE(1, 3);
    FENCE();
    const u64 tag = *(const CAS u64*)ws + TAG_STEP;
    const unsigned tagA = (unsigned)tag, tagB = (unsigned)(tag >> 32);
    const auto rsW = __builtin_amdgcn_make_buffer_rsrc(ws, 0, WS_END, 0x00020000);
    const int c2_ = 2 * xcd + (j >= 9), n = j >= 9 ? j - 9 : j;
    const auto rsCre = __builtin_amdgcn_make_buffer_rsrc((void*)(Cre + 64 * n * 576), 0, 64 * 1152, 0x00020000);
    const auto rsCim = __builtin_amdgcn_make_buffer_rsrc((void*)(Cim + 64 * n * 576), 0, 64 * 1152, 0x00020000);
    const unsigned cvo = __umul24(32 * (w & 1) + 4 * l3 + (w >> 1), 1152u) + (((lane & 7) ^ ((4 * w + (l3 >> 1)) & 7)) * 16);
#define CPIECE(q) __builtin_amdgcn_raw_ptr_buffer_load_lds((q) < 9 ? rsCre : rsCim, smem + 16384 + (q) * 8192 + w * 1024, 16, cvo, ((q) % 9) * 128, 0, 0)

    const f32x2 lr = bf2(pLr), li = bf2(pLi), dtA = bf2(pk2(exp2v(bf2(pDt) * LOG2E)));
    c2 lam = cpow(dtA * lr * LOG2E, redrev(dtA * li), sp(1.f));
    c2 l4 = csqr(csqr(lam)), l8 = csqr(l4), l16 = csqr(l8), l32 = csqr(l16);
    asm volatile("" : "+v"(lam.re), "+v"(lam.im), "+v"(l4.re), "+v"(l4.im), "+v"(l8.re), "+v"(l8.im), "+v"(l16.re), "+v"(l16.im), "+v"(l32.re), "+v"(l32.im));

    const unsigned fo = r * 128 + ((g ^ (r >> 1)) * 16);
    LAS char* pa0 = smem + wm * 4096 + fo;
    LAS char* pa1 = smem + wm * 4096 + (fo ^ 64);
    LAS char* pb0 = smem + 16384 + wn * 4096 + fo;
    LAS char* pb1 = smem + 16384 + wn * 4096 + (fo ^ 64);
    f32x4 acc[2][2][2] = {};
    v4u fr[2][12];
    STAMP(1);
#pragma unroll
    for (int s = 0; s < 10; ++s) {
      if (s <= 6) WAITV(4); else if (s <= 8) WAITV(8);
      if (s == 1) asm volatile("" ::"s"(tagA), "s"(tagB));
      BARRIER();
      if (s == 0) STAMP(2);
      if (s == 3) STAMP(11);
      if (s == 6) STAMP(12);
      if (s == 9) STAMP(13);
#pragma unroll
      for (int i = 0; i < 16; ++i) {
        if (s < 9 && i < 12) {
          const int kk = i / 6, e = i % 6;
          LAS char* pa = (kk ? pa1 : pa0) + SLOT(s);
          LAS char* pb = (kk ? pb1 : pb0) + SLOT(s);
          fr[s & 1][i] = e == 0 ? lds128(pa) : e == 5 ? lds128(pa + 2048) : lds128(pb + ((e - 1) >> 1) * 8192 + ((e - 1) & 1) * 2048);
        }
        if (s >= 1) {
          const int kk = i >> 3, m = (i >> 2) & 1, e = i & 3;
          acc[m][e >> 1][e & 1] = mfma(fr[(s - 1) & 1][6 * kk + 5 * m], fr[(s - 1) & 1][6 * kk + 1 + e], acc[m][e >> 1][e & 1]);
        }
        if ((i & 3) == 3) {
          if (s + 2 < 9) SPIECE(s + 2, i >> 2);
          if (s == 7) CPIECE(14 + (i >> 2));
          if (s == 8) CPIECE(6 + (i >> 2));
        }
        SCHED();
      }
      if (s == 6) { CPIECE(10); CPIECE(11); CPIECE(12); CPIECE(13); SCHED(); }
    }
    STAMP(3);
#define BSTEP(t, s) c2{f32x2{acc[(s) >> 1][0][t][2 * ((s) & 1)], acc[(s) >> 1][0][t][2 * ((s) & 1) + 1]}, f32x2{acc[(s) >> 1][1][t][2 * ((s) & 1)], acc[(s) >> 1][1][t][2 * ((s) & 1) + 1]}}
    c2 eA, ea, ea2;
    LAS char* sW = smem;
    LAS char* sH = smem + 2048;
    {
      c2 e2[2];
#pragma unroll
      for (int t = 0; t < 2; ++t) {
        e2[t] = BSTEP(t, 0);
#pragma unroll
        for (int s = 1; s < 4; ++s) e2[t] = cfma(el(lam, t), e2[t], BSTEP(t, s));
      }
      eA = {f32x2{e2[0].re[0], e2[1].re[0]}, f32x2{e2[0].im[0], e2[1].im[0]}};
      const c2 e = cfma(l4, eA, c2{f32x2{e2[0].re[1], e2[1].re[1]}, f32x2{e2[0].im[1], e2[1].im[1]}});
      c2 hi;
      ea.re = swap16(e.re, hi.re); ea.im = swap16(e.im, hi.im);
      const c2 A = cfma(l8, ea, hi);
      ea2.re = swap32(A.re, hi.re); ea2.im = swap32(A.im, hi.im);
      const c2 W = cfma(l16, ea2, hi);
      if (g == 0) *(LAS f32x4*)(sW + (wm * 32 + 16 * wn + r) * 16) = f32x4{W.re[0], W.re[1], W.im[0], W.im[1]};
    }
    const bool pub = w == 0 || w == 5;
    if (pub) __builtin_amdgcn_s_setprio(2);
    BARRIER();
    c2 pw;
    {
      f32x4 wv[4];
#pragma unroll
      for (int i = 0; i < 4; ++i) wv[i] = *(LAS f32x4*)(sW + (i * 32 + 16 * wn + r) * 16);
      const c2 s1 = {wv[0].xy, wv[0].zw};
      const c2 s2 = cfma(l32, s1, c2{wv[1].xy, wv[1].zw});
      const c2 s3 = cfma(l32, s2, c2{wv[2].xy, wv[2].zw});
      if (pub) {
        const c2 E = cfma(l32, s3, c2{wv[3].xy, wv[3].zw});
        if (g < 2)
          __builtin_amdgcn_raw_buffer_store_b128(v4u{__float_as_uint(g ? E.re[1] : E.re[0]), tagA, __float_as_uint(g ? E.im[1] : E.im[0]), tagB}, rsW,
                                                 WS_E + (c * 576 + p0 + g) * 16, 0, 16);
        __builtin_amdgcn_s_setprio(0);
      }
      pw.re = wm == 0 ? sp(0.f) : wm == 1 ? s1.re : wm == 2 ? s2.re : s3.re;
      pw.im = wm == 0 ? sp(0.f) : wm == 1 ? s1.im : wm == 2 ? s2.im : s3.im;
    }
    FENCE();
    STAMP(4);
    const unsigned qLr = Lre[64 * pg + lane], qLi = Lim[64 * pg + lane], qDt = ldtA[64 * pg + lane];
    FENCE();
    CPIECE(0); CPIECE(1); CPIECE(2); CPIECE(3); CPIECE(4); CPIECE(5);
    FENCE();
    const int t2 = 128 * c2_ + 32 * (w >> 1) + 4 * (w & 1) + 8 * g;
    v2u xs[4];
#pragma unroll
    for (int i = 0; i < 4; ++i) xs[i] = *(const GAS v2u*)(x + (t2 + i) * 576 + 64 * n + 4 * r);
    const v2u dsk = *(const GAS v2u*)(DA + 64 * n + 4 * r);
    FENCE();
    c2 pf2[2], lp2[2], bs[2][4];
    {
      const f32x2 den = lr * lr + li * li, inv = f32x2{__builtin_amdgcn_rcpf(den[0]), __builtin_amdgcn_rcpf(den[1])} * 2.f, nr = lam.re - 1.f;
      const c2 coef = {(nr * lr + lam.im * li) * inv, (lam.im * lr - nr * li) * inv};
      const c2 z = {sp(0.f), sp(0.f)}, one = {sp(1.f), sp(0.f)};
      const c2 lg = cmul((g & 1) ? l8 : one, (g & 2) ? l16 : one);
      const c2 lwv = cmul((wm & 1) ? l32 : one, (wm & 2) ? csqr(l32) : one);
      const c2 lw = cmul(cmul(coef, lg), lwv);
      const c2 o = (g & 2) ? ea2 : z;
      const c2 pin = (g & 1) ? cfma(l8, o, ea) : o;
      const c2 pfA = cmul(coef, cfma(lg, pw, pin));
      const c2 pfB = cfma(l4, pfA, cmul(coef, eA)), lwB = cmul(l4, lw);
#pragma unroll
      for (int t = 0; t < 2; ++t) {
        pf2[t] = {f32x2{pfA.re[t], pfB.re[t]}, f32x2{-pfA.im[t], -pfB.im[t]}};
        lp2[t] = {f32x2{lw.re[t], lwB.re[t]}, f32x2{-lw.im[t], -lwB.im[t]}};
#pragma unroll
        for (int s = 0; s < 4; ++s) {
          bs[t][s] = cmulj(el(coef, t), BSTEP(t, s));
          asm volatile("" : "+v"(bs[t][s].re), "+v"(bs[t][s].im));
        }
        asm volatile("" : "+v"(pf2[t].re), "+v"(pf2[t].im), "+v"(lp2[t].re), "+v"(lp2[t].im));
      }
    }
    if (w < 4) {
      float hr = 0.f, hi = 0.f;
      if (w < c) {
        const float dt = rbf(__builtin_amdgcn_exp2f(bflo(qDt) * LOG2E));
        const f32x2 ql2 = sp(dt * bflo(qLr) * LOG2E), qrv = redrev(sp(dt * bflo(qLi)));
        const int n0 = c - 1 - w;
        const c2 wa = cpow(ql2, qrv, f32x2{(float)(128 * n0), (float)(128 * (n0 > 4 ? n0 - 4 : 0))});
        const c2 wb = cpow(ql2, qrv, f32x2{(float)(128 * (n0 > 8 ? n0 - 8 : 0)), (float)(128 * (n0 > 12 ? n0 - 12 : 0))});
        const unsigned eo = WS_E + (w * 576 + 64 * pg + lane) * 16;
        const unsigned o1 = n0 >= 4 ? 4 * 9216 : 0, o2 = n0 >= 8 ? 8 * 9216 : 0, o3 = n0 >= 12 ? 12 * 9216 : 0;
        const f32x2 ma = {1.f, n0 >= 4 ? 1.f : 0.f}, mb = {n0 >= 8 ? 1.f : 0.f, n0 >= 12 ? 1.f : 0.f};
        c2 wa2 = {wa.re * ma, wa.im * ma}, wb2 = {wb.re * mb, wb.im * mb};
        asm volatile("" : "+v"(wa2.re), "+v"(wa2.im), "+v"(wb2.re), "+v"(wb2.im));
        v4u e0, e1, e2, e3;
#pragma unroll 1
        for (unsigned sp_ = 0; sp_ < SPIN_CAP; ++sp_) {
          FENCE();
          e0 = __builtin_amdgcn_raw_buffer_load_b128(rsW, eo, 0, 16);
          e1 = __builtin_amdgcn_raw_buffer_load_b128(rsW, eo, o1, 16);
          e2 = __builtin_amdgcn_raw_buffer_load_b128(rsW, eo, o2, 16);
          e3 = __builtin_amdgcn_raw_buffer_load_b128(rsW, eo, o3, 16);
          const unsigned bad = ((e0[1] ^ tagA) | (e0[3] ^ tagB) | (e1[1] ^ tagA)) | ((e1[3] ^ tagB) | (e2[1] ^ tagA) | (e2[3] ^ tagB)) | ((e3[1] ^ tagA) | (e3[3] ^ tagB));
          if (__all(bad == 0)) break;
          __builtin_amdgcn_s_sleep(1);
        }
        const c2 hp = cfma(wa2, c2{f32x2{__uint_as_float(e0[0]), __uint_as_float(e1[0])}, f32x2{__uint_as_float(e0[2]), __uint_as_float(e1[2])}},
                           cmul(wb2, c2{f32x2{__uint_as_float(e2[0]), __uint_as_float(e3[0])}, f32x2{__uint_as_float(e2[2]), __uint_as_float(e3[2])}}));
        hr = hp.re[0] + hp.re[1]; hi = hp.im[0] + hp.im[1];
      }
      LAS float* hq = (LAS float*)(sH + (w * 32 + (lane >> 1)) * 16) + (lane & 1);
      hq[0] = hr; hq[2] = -hi;
    }
    STAMP(5);
    BARRIER();
    {
      const f32x4 hs = (*(LAS f32x4*)(sH + (16 * wn + r) * 16) + *(LAS f32x4*)(sH + (32 + 16 * wn + r) * 16)) +
                       (*(LAS f32x4*)(sH + (64 + 16 * wn + r) * 16) + *(LAS f32x4*)(sH + (96 + 16 * wn + r) * 16));
      const c2 H = {hs.xy, hs.zw}, lamj = cj(lam);
      c2 h0 = cfma(lp2[0], el(H, 0), pf2[0]), h1 = cfma(lp2[1], el(H, 1), pf2[1]);
      unsigned dv[4][4];
#pragma unroll
      for (int s = 0; s < 4; ++s) {
        h0 = cfma(el(lamj, 0), h0, bs[0][s]);
        h1 = cfma(el(lamj, 1), h1, bs[1][s]);
        dv[0][s] = pk(h0.re[0], h1.re[0]); dv[2][s] = pk(h0.re[1], h1.re[1]);
        dv[1][s] = pk(h0.im[0], h1.im[0]); dv[3][s] = pk(h0.im[1], h1.im[1]);
      }
      LAS char* tb_ = smem + 8192 + w * 1024;
      LAS char* tw_ = tb_ + ((r >> 2) * 16 + 4 * g) * 16 + (r & 3) * 4;
      LAS char* tr_ = tb_ + (lane ^ g) * 16;
      const unsigned hoff = WS_H + (4 * c + wm) * 2 * 36864 + (2 * pg + wn) * 2048 + lane * 16;
#pragma unroll
      for (int e = 0; e < 4; ++e) {
#pragma unroll
        for (int s = 0; s < 4; ++s) *(LAS unsigned*)(tw_ + ((s ^ (r >> 2)) * 16)) = dv[e][s];
        FENCE();
        const v4u o = lds128(tr_);
        FENCE();
        __builtin_amdgcn_raw_buffer_store_b128(o, rsW, hoff + (e >> 1) * 36864 + (e & 1) * 1024, 0, 16);
      }
    }
    STAMP(6);
    WAITV(0);
    BARRIER();
    STAMP(7);
    if (w == 0) {
      if (lane == 0) __hip_atomic_store((GAS u64*)(ws + WS_FLAG) + (c * 9 + pg) * 16, tag, __ATOMIC_RELAXED, __HIP_MEMORY_SCOPE_AGENT);
      const GAS u64* fp = (const GAS u64*)(ws + WS_FLAG) + (c2_ * 9 + (lane < 9 ? lane : 0)) * 16;
#pragma unroll 1
      for (unsigned sp_ = 0; sp_ < SPIN_CAP; ++sp_) {
        FENCE();
        if (__all(__hip_atomic_load(fp, __ATOMIC_RELAXED, __HIP_MEMORY_SCOPE_AGENT) == tag)) break;
        __builtin_amdgcn_s_sleep(1);
      }
    }
    BARRIER();
    STAMP(8);
    {
      constexpr int HD = 8;
      const unsigned hsrc = WS_H + (c2_ * 8 + w) * 36864;
      v4u hf[HD];
#pragma unroll
      for (int i = 0; i < HD; ++i) hf[i] = __builtin_amdgcn_raw_buffer_load_b128(rsW, lane * 16, hsrc + i * 1024, 16);
      LAS char* pc0_ = smem + 16384 + fo;
      LAS char* pc1_ = smem + 16384 + (fo ^ 64);
      f32x4 a[4] = {};
      v4u cf[2][4];
#define CFRAG(k1, jt) lds128(((((k1) >> 1) & 1) ? pc1_ : pc0_) + (((k1) & 1) * 9 + ((k1) >> 2)) * 8192 + (jt) * 2048)
#pragma unroll
      for (int jt = 0; jt < 4; ++jt) cf[0][jt] = CFRAG(0, jt);
#pragma unroll
      for (int ks = 0; ks < 36; ++ks) {
        const v4u hc = hf[ks % HD];
#pragma unroll
        for (int jt = 0; jt < 4; ++jt) {
          if (ks + 1 < 36) cf[(ks + 1) & 1][jt] = CFRAG(ks + 1, jt);
          a[jt] = mfma(hc, cf[ks & 1][jt], a[jt]);
          SCHED();
        }
        if (ks + HD < 36) hf[ks % HD] = __builtin_amdgcn_raw_buffer_load_b128(rsW, lane * 16, hsrc + (ks + HD) * 1024, 16);
        SCHED();
        if (ks == 0) STAMP(9);
      }
      STAMP(10);
      const f32x2 d01 = bf2(dsk[0]), d23 = bf2(dsk[1]);
#pragma unroll
      for (int i = 0; i < 4; ++i) {
        const f32x2 s01 = bf2(pk2(bf2(xs[i][0]) * d01)), s23 = bf2(pk2(bf2(xs[i][1]) * d23));
        *(GAS f32x4*)(out + (t2 + i) * 1152 + 64 * n + 4 * r) = f32x4{a[0][i] + s01[0], a[1][i] + s01[1], a[2][i] + s23[0], a[3][i] + s23[1]};
      }
    }
    STAMP(15);
    STAMP_FLUSH();
  }
#endif
}

extern "C" void kernel_launch(void* const* d_in, const int* in_sizes, int n_in, void* d_out, int out_size, void* d_ws,
                              size_t ws_size, hipStream_t stream) {
  fused_ssm<<<256, 512, 0, stream>>>((const u16*)d_in[0], (const u16*)d_in[1], (const u16*)d_in[2], (const u16*)d_in[3],
                                     (const u16*)d_in[4], (const u16*)d_in[5], (const u16*)d_in[6], (const u16*)d_in[7],
                                     (const u16*)d_in[8], (const u16*)d_in[9], (const u16*)d_in[10], (const u16*)d_in[11],
                                     (const u16*)d_in[12], (const u16*)d_in[13], (const u16*)d_in[14], (const u16*)d_in[15],
                                     (float*)d_out, (char*)d_ws);
}
```

```cpp
#define AUXA 16
#define AUXB 0
#include <hip/hip_runtime.h>
#include <stdint.h>

typedef unsigned short u16;
typedef unsigned long long u64;
typedef unsigned v4u __attribute__((ext_vector_type(4)));
typedef unsigned v2u __attribute__((ext_vector_type(2)));
typedef float f32x4 __attribute__((ext_vector_type(4)));
typedef float f32x2 __attribute__((ext_vector_type(2)));
typedef __bf16 bf16x8 __attribute__((ext_vector_type(8)));
typedef __bf16 bf16x2 __attribute__((ext_vector_type(2)));
#define CAS __attribute__((address_space(4)))
#define LAS __attribute__((address_space(3)))
#define GAS __attribute__((address_space(1)))
#define DEV __device__ __forceinline__

constexpr int WS_FLAG = 4096;
constexpr int WS_E = 65536;
constexpr int WS_H = 262144;
constexpr int WS_END = WS_H + 2048 * 1152 * 2;
constexpr int WS_DBG = 8 << 20;
constexpr unsigned SPIN_CAP = 400000u;
constexpr u64 TAG_STEP = 0x9E3779B97F4A7C15ull;
#define CODE_LINES 170

#define ROTATE 0
#define PREFETCH 1
#define STAMP_ON 0
#define STAMP(i) do {} while (0)
#define STAMP_FLUSH() do {} while (0)

#define WAITV(n) asm volatile("s_waitcnt vmcnt(%0)" ::"n"(n) : "memory")
#define BARRIER() asm volatile("s_waitcnt lgkmcnt(0)\n\ts_barrier" ::: "memory")
#define FENCE() asm volatile("" ::: "memory")
#define SCHED() __builtin_amdgcn_sched_barrier(0)

struct c2 { f32x2 re, im; };
DEV f32x2 fma2(f32x2 a, f32x2 b, f32x2 c) { return __builtin_elementwise_fma(a, b, c); }
DEV f32x2 sp(float a) { return f32x2{a, a}; }
DEV c2 cmul(c2 a, c2 b) { return {fma2(a.re, b.re, -(a.im * b.im)), fma2(a.re, b.im, a.im * b.re)}; }
DEV c2 cfma(c2 a, c2 b, c2 c) { return {fma2(a.re, b.re, fma2(-a.im, b.im, c.re)), fma2(a.re, b.im, fma2(a.im, b.re, c.im))}; }
DEV c2 csqr(c2 a) { return {fma2(a.re, a.re, -(a.im * a.im)), (a.re + a.re) * a.im}; }
DEV c2 el(c2 a, int t) { return {sp(a.re[t]), sp(a.im[t])}; }
DEV float bflo(unsigned v) { return __uint_as_float(v << 16); }
DEV float bfhi(unsigned v) { return __uint_as_float(v & 0xFFFF0000u); }
DEV f32x2 bf2(unsigned v) { return f32x2{bflo(v), bfhi(v)}; }
DEV unsigned pk(float lo, float hi) { f32x2 v = {lo, hi}; return __builtin_bit_cast(unsigned, __builtin_convertvector(v, bf16x2)); }
DEV unsigned pk2(f32x2 v) { return __builtin_bit_cast(unsigned, __builtin_convertvector(v, bf16x2)); }
DEV float rbf(float f) { return bflo(pk(f, f)); }
constexpr float LOG2E = 1.44269504088896341f, INV2PI = 0.159154943091895336f;
DEV f32x2 exp2v(f32x2 x) { return f32x2{__builtin_amdgcn_exp2f(x[0]), __builtin_amdgcn_exp2f(x[1])}; }
DEV c2 cpow(f32x2 l2, f32x2 rev, f32x2 n) {
  const f32x2 m = exp2v(l2 * n), t0 = rev * n;
  const float ta = __builtin_amdgcn_fractf(t0[0]), tb = __builtin_amdgcn_fractf(t0[1]);
  return {m * f32x2{__builtin_amdgcn_cosf(ta), __builtin_amdgcn_cosf(tb)}, m * f32x2{__builtin_amdgcn_sinf(ta), __builtin_amdgcn_sinf(tb)}};
}
DEV f32x2 redrev(f32x2 ai) { const f32x2 r = ai * INV2PI; return r - f32x2{__builtin_rintf(r[0]), __builtin_rintf(r[1])}; }
DEV f32x4 mfma(v4u a, v4u b, f32x4 c) {
  return __builtin_amdgcn_mfma_f32_16x16x32_bf16(__builtin_bit_cast(bf16x8, a), __builtin_bit_cast(bf16x8, b), c, 0, 0, 0);
}
DEV v4u lds128(LAS char* p) { return *(LAS v4u*)p; }
DEV v2u lds64(LAS char* p) { return *(LAS v2u*)p; }
DEV unsigned perm(unsigned hi, unsigned lo, unsigned sel) { return __builtin_amdgcn_perm(hi, lo, sel); }
DEV f32x2 swap16(f32x2 v, f32x2& hi) {
  auto a = __builtin_amdgcn_permlane16_swap(__float_as_uint(v[0]), __float_as_uint(v[0]), false, false);
  auto b = __builtin_amdgcn_permlane16_swap(__float_as_uint(v[1]), __float_as_uint(v[1]), false, false);
  hi = f32x2{__uint_as_float(a[1]), __uint_as_float(b[1])};
  return f32x2{__uint_as_float(a[0]), __uint_as_float(b[0])};
}
DEV f32x2 swap32(f32x2 v, f32x2& hi) {
  auto a = __builtin_amdgcn_permlane32_swap(__float_as_uint(v[0]), __float_as_uint(v[0]), false, false);
  auto b = __builtin_amdgcn_permlane32_swap(__float_as_uint(v[1]), __float_as_uint(v[1]), false, false);
  hi = f32x2{__uint_as_float(a[1]), __uint_as_float(b[1])};
  return f32x2{__uint_as_float(a[0]), __uint_as_float(b[0])};
}
DEV v4u quadT(unsigned x0, unsigned x1, unsigned x2, unsigned x3, bool p, bool q) {
  const unsigned a0 = __builtin_amdgcn_mov_dpp(x0, 0xB1, 0xF, 0xF, true), a1 = __builtin_amdgcn_mov_dpp(x1, 0xB1, 0xF, 0xF, true);
  const unsigned a2 = __builtin_amdgcn_mov_dpp(x2, 0xB1, 0xF, 0xF, true), a3 = __builtin_amdgcn_mov_dpp(x3, 0xB1, 0xF, 0xF, true);
  const unsigned y0 = p ? a1 : x0, y1 = p ? x1 : a0, y2 = p ? a3 : x2, y3 = p ? x3 : a2;
  const unsigned d0 = __builtin_amdgcn_mov_dpp(y0, 0x4E, 0xF, 0xF, true), d1 = __builtin_amdgcn_mov_dpp(y1, 0x4E, 0xF, 0xF, true);
  const unsigned d2 = __builtin_amdgcn_mov_dpp(y2, 0x4E, 0xF, 0xF, true), d3 = __builtin_amdgcn_mov_dpp(y3, 0x4E, 0xF, 0xF, true);
  return v4u{q ? d2 : y0, q ? d3 : y1, q ? y2 : d0, q ? y3 : d1};
}
DEV c2 cj(c2 a) { return {a.re, -a.im}; }
DEV c2 cmulj(c2 a, c2 b) { return {fma2(a.re, b.re, -(a.im * b.im)), fma2(-a.re, b.im, -(a.im * b.re))}; }

__global__ __launch_bounds__(512, 2) void fused_ssm(
    const u16* __restrict__ x, const u16* __restrict__ Lre, const u16* __restrict__ Lim, const u16* __restrict__ Bre,
    const u16* __restrict__ Bim, const u16* __restrict__ Cre, const u16* __restrict__ Cim, const u16* __restrict__ ldtA,
    const u16* __restrict__ DA, const u16* __restrict__ u, const u16* __restrict__ lA, const u16* __restrict__ Aim,
    const u16* __restrict__ Cr, const u16* __restrict__ Ci, const u16* __restrict__ ldtB, const u16* __restrict__ DB,
    float* out, char* ws) {
#if defined(__HIP_DEVICE_COMPILE__)
  __shared__ __attribute__((aligned(16))) char smem_[163840];
  LAS char* smem = (LAS char*)smem_;
  u64 pc0;
  asm volatile("s_getpc_b64 %0" : "=s"(pc0));
  const int b = blockIdx.x, xcd = b & 7, j = b >> 3;
  const int tid = threadIdx.x, lane = tid & 63, w = __builtin_amdgcn_readfirstlane(tid >> 6);
  const int r = lane & 15, g = lane >> 4;
  STAMP(0);

  if (__builtin_expect(j >= 18, 0)) {
  if (j < 27) {
    const int d0 = 72 * xcd + 8 * (j - 18), d = d0 + w;
    LAS char* sc = smem + w * 20464;
    const int tb = ((tid >> 1) + 32 * (j - 18)) & 255, hf = tid & 1;
    const unsigned qa = *(const GAS unsigned*)(lA + d * 128 + 2 * lane), qi = *(const GAS unsigned*)(Aim + d * 128 + 2 * lane);
    const unsigned qr = *(const GAS unsigned*)(Cr + d * 128 + 2 * lane), qc = *(const GAS unsigned*)(Ci + d * 128 + 2 * lane);
    const v2u dk = *(const GAS v2u*)(DB + d0 + 4 * hf);
    const float dt = rbf(__builtin_amdgcn_exp2f(bflo(ldtB[d]) * LOG2E));
    const f32x2 l2 = bf2(pk2(exp2v(bf2(qa) * LOG2E))) * (-dt) * LOG2E, rv = redrev(bf2(qi) * dt);
    FENCE();
    STAMP(1);
    v2u uv[8];
    const auto rsU = __builtin_amdgcn_make_buffer_rsrc((void*)u, 0, 2048 * 1152, 0x00020000);
    const unsigned uvo = (8 * tb) * 1152 + (d0 + 4 * hf) * 2;
#define ULOAD(i) do { FENCE(); uv[i] = __builtin_amdgcn_raw_buffer_load_b64(rsU, uvo, (i) * 1152, 0); FENCE(); } while (0)
    ULOAD(0); ULOAD(1);
    f32x4 kacc[3] = {};
    {
      const c2 w1 = cpow(l2, rv, sp(1.f)), v8 = cpow(l2, -rv, sp(8.f)), Q = cpow(l2, rv, sp(128.f));
      LAS char* tw = sc + lane * 8;
      LAS char* tr = sc + r * 528 + g * 16;
#define PUTG(row, z) (*(LAS v2u*)(tw + (row) * 528) = v2u{pk(z.re[0], z.im[0]), pk(z.re[1], z.im[1])})
      *(LAS v2u*)(tw) = v2u{0x3F80u, 0x3F80u};
      c2 f = v8;
      PUTG(1, f);
#pragma unroll
      for (int i = 2; i < 16; ++i) { f = cmul(f, v8); PUTG(i, f); if (i == 4) ULOAD(2); if (i == 8) ULOAD(3); if (i == 12) ULOAD(4); }
      ULOAD(5);
      v4u fF[8];
#pragma unroll
      for (int ks = 0; ks < 8; ++ks) fF[ks] = lds128(tr + ks * 64);
      c2 R[8];
      R[0] = {bf2(qr) * (2.f * dt), bf2(qc) * (2.f * dt)};
#pragma unroll
      for (int i = 1; i < 8; ++i) R[i] = cmul(R[i - 1], w1);
      ULOAD(6);
#pragma unroll
      for (int i = 0; i < 8; ++i) { PUTG(i, R[i]); R[i] = cmul(R[i], Q); PUTG(8 + i, R[i]); if (i == 2) ULOAD(7); }
#pragma unroll
      for (int ks = 0; ks < 8; ++ks) kacc[0] = mfma(lds128(tr + ks * 64), fF[ks], kacc[0]);
#pragma unroll
      for (int cc = 0; cc < 4; ++cc) {
        const unsigned sel = (cc & 1) ? 0x07060302u : 0x05040100u;
        v4u o;
#pragma unroll
        for (int k = 0; k < 4; ++k) o[k] = perm(uv[2 * k + 1][cc >> 1], uv[2 * k][cc >> 1], sel);
        LAS char* ub = smem + (4 * hf + cc) * 20464 + 8448;
        *(LAS v4u*)(ub + 4640 + (8 * tb + 224) * 2) = o;
        LAS unsigned* u0 = (LAS unsigned*)(ub + (8 * tb + 226) * 2);
        u0[0] = o[0]; u0[1] = o[1]; u0[2] = o[2]; u0[3] = o[3];
      }
      STAMP(2);
      BARRIER();
      if (w < 4) __builtin_amdgcn_s_setprio(1);
#pragma unroll
      for (int s = 0; s < 2; ++s)
#pragma unroll
        for (int i = 0; i < 4; ++i) { R[2 * i] = cmul(R[2 * i], Q); PUTG(4 * s + i, R[2 * i]); }
#pragma unroll
      for (int s = 0; s < 4; ++s)
#pragma unroll
        for (int i = 0; i < 2; ++i) { R[4 * i] = cmul(R[4 * i], Q); PUTG(8 + 2 * s + i, R[4 * i]); }
#pragma unroll
      for (int ks = 0; ks < 8; ++ks) kacc[1] = mfma(lds128(tr + ks * 64), fF[ks], kacc[1]);
#pragma unroll
      for (int s = 0; s < 9; ++s) { R[0] = cmul(R[0], Q); PUTG(s, R[0]); }
#pragma unroll
      for (int ks = 0; ks < 8; ++ks) kacc[2] = mfma(lds128(tr + ks * 64), fF[ks], kacc[2]);
    }
    FENCE();
    STAMP(3);
    {
      LAS char* tt = sc + 17728;
      const unsigned aA = g < 2 ? (4 * r + 64 * g) * 2 : (128 + 2 * r + 64 * (g - 2)) * 2;
      *(LAS unsigned*)(tt + aA) = pk(kacc[1][0], kacc[1][1]);
      *(LAS unsigned*)(tt + aA + (g < 2 ? 4 : 64)) = pk(kacc[1][2], kacc[1][3]);
      const unsigned k01 = pk(kacc[2][0], kacc[2][1]), k23 = pk(kacc[2][2], kacc[2][3]);
      LAS char* t3 = tt + (256 + 64 * g + r) * 2;
      *(LAS u16*)(t3) = (u16)k01; *(LAS u16*)(t3 + 32) = (u16)(k01 >> 16);
      *(LAS u16*)(t3 + 64) = (u16)k23; *(LAS u16*)(t3 + 96) = (u16)(k23 >> 16);
      if (lane < 17) *(LAS unsigned*)(sc + (1023 + lane) * 4) = 0u;
      if (lane < 16) *(LAS unsigned*)(sc + 4224 + (1024 + lane) * 4) = 0u;
      if (lane < 28) { *(LAS v4u*)(sc + 8448 + lane * 16) = v4u{0, 0, 0, 0}; *(LAS v4u*)(sc + 8448 + 4640 + lane * 16) = v4u{0, 0, 0, 0}; }
      if (lane == 28) *(LAS unsigned*)(sc + 8448 + 448) = 0u;
      FENCE();
      const unsigned a1 = *(LAS unsigned*)(tt + lane * 4), b1 = *(LAS unsigned*)(tt + lane * 4 + 4);
      const unsigned a2 = *(LAS unsigned*)(tt + 256 + lane * 4), b2 = *(LAS unsigned*)(tt + 256 + lane * 4 + 4);
      const unsigned a3 = *(LAS unsigned*)(tt + 512 + lane * 4), b3 = *(LAS unsigned*)(tt + 512 + lane * 4 + 4);
      {
        const unsigned e0 = perm(a1, a1, 0x01000100u), m10 = perm(a1, a1, 0x01000302u), m21 = perm(a1, b1, 0x07060100u);
        *(LAS v2u*)(sc + 4224 + (1788 - 4 * lane) * 2) = v2u{m21, e0};
        *(LAS v2u*)(sc + (1788 - 4 * lane) * 2) = v2u{m10, e0};
      }
      {
        const unsigned e0 = perm(a2, a2, 0x01000100u), e1 = perm(a2, a2, 0x03020302u), e2 = perm(b2, b2, 0x01000100u);
        const unsigned m10 = perm(a2, a2, 0x01000302u), m21 = perm(a2, b2, 0x07060100u);
        *(LAS v4u*)(sc + 4224 + (1528 - 8 * lane) * 2) = v4u{e2, e1, m10, e0};
        *(LAS v4u*)(sc + (1528 - 8 * lane) * 2) = v4u{m21, e1, e0, e0};
      }
      {
        const unsigned e0 = perm(a3, a3, 0x01000100u), e1 = perm(a3, a3, 0x03020302u), e2 = perm(b3, b3, 0x01000100u);
        const unsigned m10 = perm(a3, a3, 0x01000302u), m21 = perm(a3, b3, 0x07060100u);
        LAS char* p1 = sc + 4224 + (1008 - 16 * lane) * 2;
        LAS char* p0_ = sc + (1008 - 16 * lane) * 2;
        *(LAS v4u*)(p1) = v4u{e2, e2, e1, e1}; *(LAS v4u*)(p1 + 16) = v4u{e1, m10, e0, e0};
        *(LAS v4u*)(p0_) = v4u{e2, m21, e1, e1}; *(LAS v4u*)(p0_ + 16) = v4u{e1, e0, e0, e0};
      }
      FENCE();
      {
        const int q0 = 8 * r + 4 * (g & 1) + 128 * (g >> 1);
        const unsigned v32 = pk(kacc[0][3], kacc[0][2]), v10 = pk(kacc[0][1], kacc[0][0]), v21 = pk(kacc[0][2], kacc[0][1]);
        *(LAS v2u*)(sc + 4224 + (2044 - q0) * 2) = v2u{v32, v10};
        *(LAS u16*)(sc + (2043 - q0) * 2) = (u16)v32;
        *(LAS unsigned*)(sc + (2044 - q0) * 2) = v21;
        *(LAS u16*)(sc + (2046 - q0) * 2) = (u16)(v10 >> 16);
      }
    }
    FENCE();
    STAMP(4);
    {
      f32x4 ya[8] = {};
      LAS char* pT = sc + (r & 1) * 4224 + 504 - 8 * (r >> 1) + 16 * g;
      LAS char* pT2 = pT + 8;
      asm volatile("" : "+v"(pT2));
      LAS char* pU = sc + 8448 + (r & 1) * 4640 + 448 + 64 * (r >> 1) + 16 * g;
      v4u U[8], Tq[2];
#define LDT(off) ({ const v2u a_ = lds64(pT + (off)), b_ = lds64(pT2 + (off)); v4u{a_[0], a_[1], b_[0], b_[1]}; })
      Tq[0] = LDT(512 * 7);
#pragma unroll
      for (int k = 7; k >= 0; --k) U[k] = lds128(pU + 512 * k);
      Tq[1] = LDT(512 * 6);
#pragma unroll 1
      for (int rr = 0; rr < 8; ++rr) {
#pragma unroll
        for (int m = 0; m < 8; ++m) {
          const v4u T = Tq[m & 1];
          Tq[m & 1] = m < 6 ? LDT(512 * (5 - m)) : LDT(512 * (13 - m) - 64);
#pragma unroll
          for (int k = 7 - m; k >= 0; --k) ya[k + m] = mfma(T, U[k], ya[k + m]);
          U[7 - m] = lds128(pU + 512 * (7 - m) - 64);
          SCHED();
        }
        pT -= 64; pT2 -= 64; pU -= 64;
      }
      FENCE();
      LAS char* py = sc + (4 * g + (r & 1) + 16 * (r >> 1)) * 4;
#pragma unroll
      for (int q = 0; q < 8; ++q) {
        *(LAS unsigned*)(py + 512 * q) = pk(ya[q][0], ya[q][1]);
        *(LAS unsigned*)(py + 512 * q + 8) = pk(ya[q][2], ya[q][3]);
      }
    }
    STAMP(5);
    BARRIER();
    {
      v4u yv[4];
#pragma unroll
      for (int cc = 0; cc < 4; ++cc) yv[cc] = lds128(smem + (4 * hf + cc) * 20464 + 16 * tb);
      const auto rsO = __builtin_amdgcn_make_buffer_rsrc(out, 0, 2048 * 1152 * 4, 0x00020000);
      const unsigned op = ((8 * tb) * 1152 + 576 + d0 + 4 * hf) * 4;
      const f32x2 d01 = bf2(dk[0]), d23 = bf2(dk[1]);
#pragma unroll
      for (int i = 0; i < 8; ++i) {
        const f32x2 s01 = bf2(pk2(bf2(uv[i][0]) * d01)), s23 = bf2(pk2(bf2(uv[i][1]) * d23));
        f32x2 y01, y23;
        if (i & 1) { y01 = f32x2{bfhi(yv[0][i >> 1]), bfhi(yv[1][i >> 1])}; y23 = f32x2{bfhi(yv[2][i >> 1]), bfhi(yv[3][i >> 1])}; }
        else { y01 = f32x2{bflo(yv[0][i >> 1]), bflo(yv[1][i >> 1])}; y23 = f32x2{bflo(yv[2][i >> 1]), bflo(yv[3][i >> 1])}; }
        const f32x2 o01 = bf2(pk2(y01 + s01)), o23 = bf2(pk2(y23 + s23));
        __builtin_amdgcn_raw_buffer_store_b128(__builtin_bit_cast(v4u, f32x4{o01[0], o01[1], o23[0], o23[1]}), rsO, op + i * 4608, 0, AUXB);
      }
    }
    STAMP(6);
    STAMP(15);
    STAMP_FLUSH();
  } else if (b != 255) {
    unsigned v = 0;
    if (j == 27 && tid < CODE_LINES) v = *(const GAS unsigned*)((pc0 & ~127ull) + tid * 128);
    if (j < 31) {
      const int R = ((j - 27) * 8 + w) * 36 + (lane < 36 ? lane : 35);
      const u64 bx = (u64)x + (xcd >> 1) * 512 * 1152, br = (u64)Bre + ((xcd & 1) * 256 - 512) * 1152, bi = (u64)Bim + ((xcd & 1) * 256 - 832) * 1152;
      const u64 base = (R < 512 ? bx : (R < 832 ? br : bi)) + R * 1152;
      unsigned t[9];
#pragma unroll
      for (int k = 0; k < 9; ++k) t[k] = *(const GAS unsigned*)(base + k * 128);
#pragma unroll
      for (int k = 0; k < 9; ++k) v ^= t[k];
    }
    asm volatile("" ::"v"(v));
  } else if (w == 0) {
    const u64 tag = *(const CAS u64*)ws + TAG_STEP;
    const GAS u64* fl = (const GAS u64*)(ws + WS_FLAG);
#pragma unroll 1
    for (unsigned sp_ = 0; sp_ < SPIN_CAP; ++sp_) {
      __builtin_amdgcn_s_sleep(16);
      FENCE();
      const u64 f0 = __hip_atomic_load(fl + lane * 16, __ATOMIC_RELAXED, __HIP_MEMORY_SCOPE_AGENT);
      const u64 f1 = __hip_atomic_load(fl + (64 + lane) * 16, __ATOMIC_RELAXED, __HIP_MEMORY_SCOPE_AGENT);
      const u64 f2 = __hip_atomic_load(fl + (128 + (lane & 15)) * 16, __ATOMIC_RELAXED, __HIP_MEMORY_SCOPE_AGENT);
      if (__all(f0 == tag && f1 == tag && f2 == tag)) break;
    }
    if (lane == 0) __hip_atomic_store((GAS u64*)ws, tag, __ATOMIC_RELAXED, __HIP_MEMORY_SCOPE_AGENT);
  }
  return;
  }
  {
    const int odd = xcd & 1, mid = j >> 4;
    const int pi = mid ? (j - 16) + 2 * odd : (j >> 2), gi = mid ? 4 : (j & 3);
    const int pg = mid ? 4 : gi + 5 * odd, c = 4 * (xcd >> 1) + pi;
    const int wm = w & 3, wn = w >> 2;
    const int p0 = 64 * pg + 32 * wn + 2 * r;
    const unsigned pLr = *(const GAS unsigned*)(Lre + p0), pLi = *(const GAS unsigned*)(Lim + p0), pDt = *(const GAS unsigned*)(ldtA + p0);
    FENCE();
    const u64 sx = (u64)x + (c * 128 + w * 32) * 1152, sb = (u64)(w < 6 ? Bre : Bim) + (pg * 64 + (w & 1) * 32) * 1152;
    const auto rsS = __builtin_amdgcn_make_buffer_rsrc((void*)(wn ? sb : sx), 0, 32 * 1152, 0x00020000);
    const int l3 = lane >> 3, ch = (lane & 7) ^ (l3 >> 1);
    const int rowx = 8 * (l3 >> 2) + 4 * (l3 & 1) + ((l3 >> 1) & 1), rowb = rowx ^ ((rowx ^ (2 * l3)) & -wn);
    const unsigned dvA = __umul24(rowb, 1152u) + ch * 16, dvB = dvA + 16 * 1152 + 64 - 2 * (dvA & 64);
    const int stp = (2 - wn) * 1152;
#define SLOT(s) (((8 - (s)) % 3) * 32768)
#define SPIECE(s, i) __builtin_amdgcn_raw_ptr_buffer_load_lds(rsS, smem + SLOT(s) + w * 4096 + (i) * 1024, 16, ((i) & 1) ? dvB : dvA, (s) * 128 + ((i) >> 1) * stp, 0, 0)
    SPIECE(0, 0); SPIECE(0, 1); SPIECE(0, 2); SPIECE(0, 3);
    asm volatile("s_barrier" ::: "memory");
    SPIECE(1, 0); SPIECE(1, 1); SPIECE(1, 2); SPIECE(1, 3);
    FENCE();
    const u64 tag = *(const CAS u64*)ws + TAG_STEP;
    const unsigned tagA = (unsigned)tag, tagB = (unsigned)(tag >> 32);
    const auto rsW = __builtin_amdgcn_make_buffer_rsrc(ws, 0, WS_END, 0x00020000);
    const int c2_ = 2 * xcd + (j >= 9), n = j >= 9 ? j - 9 : j;
    const auto rsCre = __builtin_amdgcn_make_buffer_rsrc((void*)(Cre + 64 * n * 576), 0, 64 * 1152, 0x00020000);
    const auto rsCim = __builtin_amdgcn_make_buffer_rsrc((void*)(Cim + 64 * n * 576), 0, 64 * 1152, 0x00020000);
    const unsigned cvo = __umul24(32 * (w & 1) + 4 * l3 + (w >> 1), 1152u) + (((lane & 7) ^ ((4 * w + (l3 >> 1)) & 7)) * 16);
#define CPIECE(q) __builtin_amdgcn_raw_ptr_buffer_load_lds((q) < 9 ? rsCre : rsCim, smem + 16384 + (q) * 8192 + w * 1024, 16, cvo, ((q) % 9) * 128, 0, 0)

    const f32x2 lr = bf2(pLr), li = bf2(pLi), dtA = bf2(pk2(exp2v(bf2(pDt) * LOG2E)));
    c2 lam = cpow(dtA * lr * LOG2E, redrev(dtA * li), sp(1.f));
    c2 l4 = csqr(csqr(lam)), l8 = csqr(l4), l16 = csqr(l8), l32 = csqr(l16);
    asm volatile("" : "+v"(lam.re), "+v"(lam.im), "+v"(l4.re), "+v"(l4.im), "+v"(l8.re), "+v"(l8.im), "+v"(l16.re), "+v"(l16.im), "+v"(l32.re), "+v"(l32.im));

    const unsigned fo = r * 128 + ((g ^ (r >> 1)) * 16);
    LAS char* pa0 = smem + wm * 4096 + fo;
    LAS char* pa1 = smem + wm * 4096 + (fo ^ 64);
    LAS char* pb0 = smem + 16384 + wn * 4096 + fo;
    LAS char* pb1 = smem + 16384 + wn * 4096 + (fo ^ 64);
    f32x4 acc[2][2][2] = {};
    v4u fr[2][12];
    STAMP(1);
#pragma unroll
    for (int s = 0; s < 10; ++s) {
      if (s <= 6) WAITV(4); else if (s <= 8) WAITV(8);
      if (s == 1) asm volatile("" ::"s"(tagA), "s"(tagB));
      BARRIER();
      if (s == 0) STAMP(2);
      if (s == 3) STAMP(11);
      if (s == 6) STAMP(12);
      if (s == 9) STAMP(13);
#pragma unroll
      for (int i = 0; i < 16; ++i) {
        if (s < 9 && i < 12) {
          const int kk = i / 6, e = i % 6;
          LAS char* pa = (kk ? pa1 : pa0) + SLOT(s);
          LAS char* pb = (kk ? pb1 : pb0) + SLOT(s);
          fr[s & 1][i] = e == 0 ? lds128(pa) : e == 5 ? lds128(pa + 2048) : lds128(pb + ((e - 1) >> 1) * 8192 + ((e - 1) & 1) * 2048);
        }
        if (s >= 1) {
          const int kk = i >> 3, m = (i >> 2) & 1, e = i & 3;
          acc[m][e >> 1][e & 1] = mfma(fr[(s - 1) & 1][6 * kk + 5 * m], fr[(s - 1) & 1][6 * kk + 1 + e], acc[m][e >> 1][e & 1]);
        }
        if ((i & 3) == 3) {
          if (s + 2 < 9) SPIECE(s + 2, i >> 2);
          if (s == 7) CPIECE(14 + (i >> 2));
          if (s == 8) CPIECE(6 + (i >> 2));
        }
        SCHED();
      }
      if (s == 6) { CPIECE(10); CPIECE(11); CPIECE(12); CPIECE(13); SCHED(); }
    }
    STAMP(3);
#define BSTEP(t, s) c2{f32x2{acc[(s) >> 1][0][t][2 * ((s) & 1)], acc[(s) >> 1][0][t][2 * ((s) & 1) + 1]}, f32x2{acc[(s) >> 1][1][t][2 * ((s) & 1)], acc[(s) >> 1][1][t][2 * ((s) & 1) + 1]}}
    c2 eA, ea, ea2;
    LAS char* sW = smem;
    LAS char* sH = smem + 2048;
    {
      c2 e2[2];
#pragma unroll
      for (int t = 0; t < 2; ++t) {
        e2[t] = BSTEP(t, 0);
#pragma unroll
        for (int s = 1; s < 4; ++s) e2[t] = cfma(el(lam, t), e2[t], BSTEP(t, s));
      }
      eA = {f32x2{e2[0].re[0], e2[1].re[0]}, f32x2{e2[0].im[0], e2[1].im[0]}};
      const c2 e = cfma(l4, eA, c2{f32x2{e2[0].re[1], e2[1].re[1]}, f32x2{e2[0].im[1], e2[1].im[1]}});
      c2 hi;
      ea.re = swap16(e.re, hi.re); ea.im = swap16(e.im, hi.im);
      const c2 A = cfma(l8, ea, hi);
      ea2.re = swap32(A.re, hi.re); ea2.im = swap32(A.im, hi.im);
      const c2 W = cfma(l16, ea2, hi);
      if (g == 0) *(LAS f32x4*)(sW + (wm * 32 + 16 * wn + r) * 16) = f32x4{W.re[0], W.re[1], W.im[0], W.im[1]};
    }
    const bool pub = w == 0 || w == 5;
    if (pub) __builtin_amdgcn_s_setprio(2);
    BARRIER();
    c2 pw;
    {
      f32x4 wv[4];
#pragma unroll
      for (int i = 0; i < 4; ++i) wv[i] = *(LAS f32x4*)(sW + (i * 32 + 16 * wn + r) * 16);
      const c2 s1 = {wv[0].xy, wv[0].zw};
      const c2 s2 = cfma(l32, s1, c2{wv[1].xy, wv[1].zw});
      const c2 s3 = cfma(l32, s2, c2{wv[2].xy, wv[2].zw});
      if (pub) {
        const c2 E = cfma(l32, s3, c2{wv[3].xy, wv[3].zw});
        if (g < 2)
          __builtin_amdgcn_raw_buffer_store_b128(v4u{__float_as_uint(g ? E.re[1] : E.re[0]), tagA, __float_as_uint(g ? E.im[1] : E.im[0]), tagB}, rsW,
                                                 WS_E + (c * 576 + p0 + g) * 16, 0, 16);
        __builtin_amdgcn_s_setprio(0);
      }
      pw.re = wm == 0 ? sp(0.f) : wm == 1 ? s1.re : wm == 2 ? s2.re : s3.re;
      pw.im = wm == 0 ? sp(0.f) : wm == 1 ? s1.im : wm == 2 ? s2.im : s3.im;
    }
    FENCE();
    STAMP(4);
    const unsigned qLr = Lre[64 * pg + lane], qLi = Lim[64 * pg + lane], qDt = ldtA[64 * pg + lane];
    FENCE();
    CPIECE(0); CPIECE(1); CPIECE(2); CPIECE(3); CPIECE(4); CPIECE(5);
    FENCE();
    const int t2 = 128 * c2_ + 32 * (w >> 1) + 4 * (w & 1) + 8 * g;
    v2u xs[4];
#pragma unroll
    for (int i = 0; i < 4; ++i) xs[i] = *(const GAS v2u*)(x + (t2 + i) * 576 + 64 * n + 4 * r);
    const v2u dsk = *(const GAS v2u*)(DA + 64 * n + 4 * r);
    FENCE();
    c2 pf2[2], lp2[2], bs[2][4];
    {
      const f32x2 den = lr * lr + li * li, inv = f32x2{__builtin_amdgcn_rcpf(den[0]), __builtin_amdgcn_rcpf(den[1])} * 2.f, nr = lam.re - 1.f;
      const c2 coef = {(nr * lr + lam.im * li) * inv, (lam.im * lr - nr * li) * inv};
      const c2 z = {sp(0.f), sp(0.f)}, one = {sp(1.f), sp(0.f)};
      const c2 lg = cmul((g & 1) ? l8 : one, (g & 2) ? l16 : one);
      const c2 lwv = cmul((wm & 1) ? l32 : one, (wm & 2) ? csqr(l32) : one);
      const c2 lw = cmul(cmul(coef, lg), lwv);
      const c2 o = (g & 2) ? ea2 : z;
      const c2 pin = (g & 1) ? cfma(l8, o, ea) : o;
      const c2 pfA = cmul(coef, cfma(lg, pw, pin));
      const c2 pfB = cfma(l4, pfA, cmul(coef, eA)), lwB = cmul(l4, lw);
#pragma unroll
      for (int t = 0; t < 2; ++t) {
        pf2[t] = {f32x2{pfA.re[t], pfB.re[t]}, f32x2{-pfA.im[t], -pfB.im[t]}};
        lp2[t] = {f32x2{lw.re[t], lwB.re[t]}, f32x2{-lw.im[t], -lwB.im[t]}};
#pragma unroll
        for (int s = 0; s < 4; ++s) {
          bs[t][s] = cmulj(el(coef, t), BSTEP(t, s));
          asm volatile("" : "+v"(bs[t][s].re), "+v"(bs[t][s].im));
        }
        asm volatile("" : "+v"(pf2[t].re), "+v"(pf2[t].im), "+v"(lp2[t].re), "+v"(lp2[t].im));
      }
    }
    if (w < 4) {
      float hr = 0.f, hi = 0.f;
      if (w < c) {
        const float dt = rbf(__builtin_amdgcn_exp2f(bflo(qDt) * LOG2E));
        const f32x2 ql2 = sp(dt * bflo(qLr) * LOG2E), qrv = redrev(sp(dt * bflo(qLi)));
        const int n0 = c - 1 - w;
        const c2 wa = cpow(ql2, qrv, f32x2{(float)(128 * n0), (float)(128 * (n0 > 4 ? n0 - 4 : 0))});
        const c2 wb = cpow(ql2, qrv, f32x2{(float)(128 * (n0 > 8 ? n0 - 8 : 0)), (float)(128 * (n0 > 12 ? n0 - 12 : 0))});
        const unsigned eo = WS_E + (w * 576 + 64 * pg + lane) * 16;
        const unsigned o1 = n0 >= 4 ? 4 * 9216 : 0, o2 = n0 >= 8 ? 8 * 9216 : 0, o3 = n0 >= 12 ? 12 * 9216 : 0;
        const f32x2 ma = {1.f, n0 >= 4 ? 1.f : 0.f}, mb = {n0 >= 8 ? 1.f : 0.f, n0 >= 12 ? 1.f : 0.f};
        c2 wa2 = {wa.re * ma, wa.im * ma}, wb2 = {wb.re * mb, wb.im * mb};
        asm volatile("" : "+v"(wa2.re), "+v"(wa2.im), "+v"(wb2.re), "+v"(wb2.im));
        v4u e0, e1, e2, e3;
#pragma unroll 1
        for (unsigned sp_ = 0; sp_ < SPIN_CAP; ++sp_) {
          FENCE();
          e0 = __builtin_amdgcn_raw_buffer_load_b128(rsW, eo, 0, 16);
          e1 = __builtin_amdgcn_raw_buffer_load_b128(rsW, eo, o1, 16);
          e2 = __builtin_amdgcn_raw_buffer_load_b128(rsW, eo, o2, 16);
          e3 = __builtin_amdgcn_raw_buffer_load_b128(rsW, eo, o3, 16);
          const unsigned bad = ((e0[1] ^ tagA) | (e0[3] ^ tagB) | (e1[1] ^ tagA)) | ((e1[3] ^ tagB) | (e2[1] ^ tagA) | (e2[3] ^ tagB)) | ((e3[1] ^ tagA) | (e3[3] ^ tagB));
          if (__all(bad == 0)) break;
          __builtin_amdgcn_s_sleep(1);
        }
        const c2 hp = cfma(wa2, c2{f32x2{__uint_as_float(e0[0]), __uint_as_float(e1[0])}, f32x2{__uint_as_float(e0[2]), __uint_as_float(e1[2])}},
                           cmul(wb2, c2{f32x2{__uint_as_float(e2[0]), __uint_as_float(e3[0])}, f32x2{__uint_as_float(e2[2]), __uint_as_float(e3[2])}}));
        hr = hp.re[0] + hp.re[1]; hi = hp.im[0] + hp.im[1];
      }
      LAS float* hq = (LAS float*)(sH + (w * 32 + (lane >> 1)) * 16) + (lane & 1);
      hq[0] = hr; hq[2] = -hi;
    }
    STAMP(5);
    BARRIER();
    {
      const f32x4 hs = (*(LAS f32x4*)(sH + (16 * wn + r) * 16) + *(LAS f32x4*)(sH + (32 + 16 * wn + r) * 16)) +
                       (*(LAS f32x4*)(sH + (64 + 16 * wn + r) * 16) + *(LAS f32x4*)(sH + (96 + 16 * wn + r) * 16));
      const c2 H = {hs.xy, hs.zw}, lamj = cj(lam);
      c2 h0 = cfma(lp2[0], el(H, 0), pf2[0]), h1 = cfma(lp2[1], el(H, 1), pf2[1]);
      unsigned dv[4][4];
#pragma unroll
      for (int s = 0; s < 4; ++s) {
        h0 = cfma(el(lamj, 0), h0, bs[0][s]);
        h1 = cfma(el(lamj, 1), h1, bs[1][s]);
        dv[0][s] = pk(h0.re[0], h1.re[0]); dv[2][s] = pk(h0.re[1], h1.re[1]);
        dv[1][s] = pk(h0.im[0], h1.im[0]); dv[3][s] = pk(h0.im[1], h1.im[1]);
      }
      LAS char* tb_ = smem + 8192 + w * 1024;
      LAS char* tw_ = tb_ + ((r >> 2) * 16 + 4 * g) * 16 + (r & 3) * 4;
      LAS char* tr_ = tb_ + (lane ^ g) * 16;
      const unsigned hoff = WS_H + (4 * c + wm) * 2 * 36864 + (2 * pg + wn) * 2048 + lane * 16;
#pragma unroll
      for (int e = 0; e < 4; ++e) {
#pragma unroll
        for (int s = 0; s < 4; ++s) *(LAS unsigned*)(tw_ + ((s ^ (r >> 2)) * 16)) = dv[e][s];
        FENCE();
        const v4u o = lds128(tr_);
        FENCE();
        __builtin_amdgcn_raw_buffer_store_b128(o, rsW, hoff + (e >> 1) * 36864 + (e & 1) * 1024, 0, 16);
      }
    }
    STAMP(6);
    WAITV(0);
    BARRIER();
    STAMP(7);
    if (w == 0) {
      if (lane == 0) __hip_atomic_store((GAS u64*)(ws + WS_FLAG) + (c * 9 + pg) * 16, tag, __ATOMIC_RELAXED, __HIP_MEMORY_SCOPE_AGENT);
      const GAS u64* fp = (const GAS u64*)(ws + WS_FLAG) + (c2_ * 9 + (lane < 9 ? lane : 0)) * 16;
#pragma unroll 1
      for (unsigned sp_ = 0; sp_ < SPIN_CAP; ++sp_) {
        FENCE();
        if (__all(__hip_atomic_load(fp, __ATOMIC_RELAXED, __HIP_MEMORY_SCOPE_AGENT) == tag)) break;
        __builtin_amdgcn_s_sleep(1);
      }
    }
    BARRIER();
    STAMP(8);
    {
      constexpr int HD = 8;
      const unsigned hsrc = WS_H + (c2_ * 8 + w) * 36864;
      v4u hf[HD];
#pragma unroll
      for (int i = 0; i < HD; ++i) hf[i] = __builtin_amdgcn_raw_buffer_load_b128(rsW, lane * 16, hsrc + i * 1024, 16);
      LAS char* pc0_ = smem + 16384 + fo;
      LAS char* pc1_ = smem + 16384 + (fo ^ 64);
      f32x4 a[4] = {};
      v4u cf[2][4];
#define CFRAG(k1, jt) lds128(((((k1) >> 1) & 1) ? pc1_ : pc0_) + (((k1) & 1) * 9 + ((k1) >> 2)) * 8192 + (jt) * 2048)
#pragma unroll
      for (int jt = 0; jt < 4; ++jt) cf[0][jt] = CFRAG(0, jt);
#pragma unroll
      for (int ks = 0; ks < 36; ++ks) {
        const v4u hc = hf[ks % HD];
#pragma unroll
        for (int jt = 0; jt < 4; ++jt) {
          if (ks + 1 < 36) cf[(ks + 1) & 1][jt] = CFRAG(ks + 1, jt);
          a[jt] = mfma(hc, cf[ks & 1][jt], a[jt]);
          SCHED();
        }
        if (ks + HD < 36) hf[ks % HD] = __builtin_amdgcn_raw_buffer_load_b128(rsW, lane * 16, hsrc + (ks + HD) * 1024, 16);
        SCHED();
        if (ks == 0) STAMP(9);
      }
      STAMP(10);
      const auto rsO = __builtin_amdgcn_make_buffer_rsrc(out, 0, 2048 * 1152 * 4, 0x00020000);
      const f32x2 d01 = bf2(dsk[0]), d23 = bf2(dsk[1]);
#pragma unroll
      for (int i = 0; i < 4; ++i) {
        const f32x2 s01 = bf2(pk2(bf2(xs[i][0]) * d01)), s23 = bf2(pk2(bf2(xs[i][1]) * d23));
        __builtin_amdgcn_raw_buffer_store_b128(__builtin_bit_cast(v4u, f32x4{a[0][i] + s01[0], a[1][i] + s01[1], a[2][i] + s23[0], a[3][i] + s23[1]}), rsO,
                                               ((t2 + i) * 1152 + 64 * n + 4 * r) * 4, 0, AUXA);
      }
    }
    STAMP(15);
    STAMP_FLUSH();
  }
#endif
}

extern "C" void kernel_launch(void* const* d_in, const int* in_sizes, int n_in, void* d_out, int out_size, void* d_ws,
                              size_t ws_size, hipStream_t stream) {
  fused_ssm<<<256, 512, 0, stream>>>((const u16*)d_in[0], (const u16*)d_in[1], (const u16*)d_in[2], (const u16*)d_in[3],
                                     (const u16*)d_in[4], (const u16*)d_in[5], (const u16*)d_in[6], (const u16*)d_in[7],
                                     (const u16*)d_in[8], (const u16*)d_in[9], (const u16*)d_in[10], (const u16*)d_in[11],
                                     (const u16*)d_in[12], (const u16*)d_in[13], (const u16*)d_in[14], (const u16*)d_in[15],
                                     (float*)d_out, (char*)d_ws);
}
```

```cpp
#define STAMP_ON 1
#define AMP_K 14
#define AMP_ROLE 0
#define AMP_F 0
#define AMP_W 7
#define AMP_FIX 0
#include <hip/hip_runtime.h>
#include <stdint.h>

typedef unsigned short u16;
typedef unsigned long long u64;
typedef unsigned v4u __attribute__((ext_vector_type(4)));
typedef unsigned v2u __attribute__((ext_vector_type(2)));
typedef float f32x4 __attribute__((ext_vector_type(4)));
typedef float f32x2 __attribute__((ext_vector_type(2)));
typedef __bf16 bf16x8 __attribute__((ext_vector_type(8)));
typedef __bf16 bf16x2 __attribute__((ext_vector_type(2)));
#define CAS __attribute__((address_space(4)))
#define LAS __attribute__((address_space(3)))
#define GAS __attribute__((address_space(1)))
#define DEV __device__ __forceinline__

constexpr int WS_FLAG = 4096;
constexpr int WS_E = 65536;
constexpr int WS_H = 262144;
constexpr int WS_END = WS_H + 2048 * 1152 * 2;
constexpr int WS_DBG = 8 << 20;
constexpr unsigned SPIN_CAP = 400000u;
constexpr u64 TAG_STEP = 0x9E3779B97F4A7C15ull;
#define CODE_LINES 170

#define ROTATE 0
#define PREFETCH 1
#define AUXA 0
#define AUXB 0
#define STAMP(i) do { __builtin_amdgcn_sched_barrier(0); asm volatile("s_memrealtime %0\n\ts_waitcnt lgkmcnt(0)" : "=s"(stamp[i]) :: "memory"); __builtin_amdgcn_sched_barrier(0); } while (0)
#define STAMP_FLUSH() do { if (lane == 0 && w == AMP_W) { GAS u64* sp = (GAS u64*)(ws + WS_DBG) + b * 16; const u64 tg_ = *(const CAS u64*)ws + TAG_STEP; \
    __hip_atomic_store(sp + 0, stamp[0], __ATOMIC_RELAXED, __HIP_MEMORY_SCOPE_AGENT); __hip_atomic_store(sp + 1, stamp[AMP_K], __ATOMIC_RELAXED, __HIP_MEMORY_SCOPE_AGENT); \
    WAITV(0); __hip_atomic_store(sp + 2, tg_, __ATOMIC_RELAXED, __HIP_MEMORY_SCOPE_AGENT); } } while (0)

#define WAITV(n) asm volatile("s_waitcnt vmcnt(%0)" ::"n"(n) : "memory")
#define BARRIER() asm volatile("s_waitcnt lgkmcnt(0)\n\ts_barrier" ::: "memory")
#define FENCE() asm volatile("" ::: "memory")
#define SCHED() __builtin_amdgcn_sched_barrier(0)

struct c2 { f32x2 re, im; };
DEV f32x2 fma2(f32x2 a, f32x2 b, f32x2 c) { return __builtin_elementwise_fma(a, b, c); }
DEV f32x2 sp(float a) { return f32x2{a, a}; }
DEV c2 cmul(c2 a, c2 b) { return {fma2(a.re, b.re, -(a.im * b.im)), fma2(a.re, b.im, a.im * b.re)}; }
DEV c2 cfma(c2 a, c2 b, c2 c) { return {fma2(a.re, b.re, fma2(-a.im, b.im, c.re)), fma2(a.re, b.im, fma2(a.im, b.re, c.im))}; }
DEV c2 csqr(c2 a) { return {fma2(a.re, a.re, -(a.im * a.im)), (a.re + a.re) * a.im}; }
DEV c2 el(c2 a, int t) { return {sp(a.re[t]), sp(a.im[t])}; }
DEV float bflo(unsigned v) { return __uint_as_float(v << 16); }
DEV float bfhi(unsigned v) { return __uint_as_float(v & 0xFFFF0000u); }
DEV f32x2 bf2(unsigned v) { return f32x2{bflo(v), bfhi(v)}; }
DEV unsigned pk(float lo, float hi) { f32x2 v = {lo, hi}; return __builtin_bit_cast(unsigned, __builtin_convertvector(v, bf16x2)); }
DEV unsigned pk2(f32x2 v) { return __builtin_bit_cast(unsigned, __builtin_convertvector(v, bf16x2)); }
DEV float rbf(float f) { return bflo(pk(f, f)); }
constexpr float LOG2E = 1.44269504088896341f, INV2PI = 0.159154943091895336f;
DEV f32x2 exp2v(f32x2 x) { return f32x2{__builtin_amdgcn_exp2f(x[0]), __builtin_amdgcn_exp2f(x[1])}; }
DEV c2 cpow(f32x2 l2, f32x2 rev, f32x2 n) {
  const f32x2 m = exp2v(l2 * n), t0 = rev * n;
  const float ta = __builtin_amdgcn_fractf(t0[0]), tb = __builtin_amdgcn_fractf(t0[1]);
  return {m * f32x2{__builtin_amdgcn_cosf(ta), __builtin_amdgcn_cosf(tb)}, m * f32x2{__builtin_amdgcn_sinf(ta), __builtin_amdgcn_sinf(tb)}};
}
DEV f32x2 redrev(f32x2 ai) { const f32x2 r = ai * INV2PI; return r - f32x2{__builtin_rintf(r[0]), __builtin_rintf(r[1])}; }
DEV f32x4 mfma(v4u a, v4u b, f32x4 c) {
  return __builtin_amdgcn_mfma_f32_16x16x32_bf16(__builtin_bit_cast(bf16x8, a), __builtin_bit_cast(bf16x8, b), c, 0, 0, 0);
}
DEV v4u lds128(LAS char* p) { return *(LAS v4u*)p; }
DEV v2u lds64(LAS char* p) { return *(LAS v2u*)p; }
DEV unsigned perm(unsigned hi, unsigned lo, unsigned sel) { return __builtin_amdgcn_perm(hi, lo, sel); }
DEV f32x2 swap16(f32x2 v, f32x2& hi) {
  auto a = __builtin_amdgcn_permlane16_swap(__float_as_uint(v[0]), __float_as_uint(v[0]), false, false);
  auto b = __builtin_amdgcn_permlane16_swap(__float_as_uint(v[1]), __float_as_uint(v[1]), false, false);
  hi = f32x2{__uint_as_float(a[1]), __uint_as_float(b[1])};
  return f32x2{__uint_as_float(a[0]), __uint_as_float(b[0])};
}
DEV f32x2 swap32(f32x2 v, f32x2& hi) {
  auto a = __builtin_amdgcn_permlane32_swap(__float_as_uint(v[0]), __float_as_uint(v[0]), false, false);
  auto b = __builtin_amdgcn_permlane32_swap(__float_as_uint(v[1]), __float_as_uint(v[1]), false, false);
  hi = f32x2{__uint_as_float(a[1]), __uint_as_float(b[1])};
  return f32x2{__uint_as_float(a[0]), __uint_as_float(b[0])};
}
DEV v4u quadT(unsigned x0, unsigned x1, unsigned x2, unsigned x3, bool p, bool q) {
  const unsigned a0 = __builtin_amdgcn_mov_dpp(x0, 0xB1, 0xF, 0xF, true), a1 = __builtin_amdgcn_mov_dpp(x1, 0xB1, 0xF, 0xF, true);
  const unsigned a2 = __builtin_amdgcn_mov_dpp(x2, 0xB1, 0xF, 0xF, true), a3 = __builtin_amdgcn_mov_dpp(x3, 0xB1, 0xF, 0xF, true);
  const unsigned y0 = p ? a1 : x0, y1 = p ? x1 : a0, y2 = p ? a3 : x2, y3 = p ? x3 : a2;
  const unsigned d0 = __builtin_amdgcn_mov_dpp(y0, 0x4E, 0xF, 0xF, true), d1 = __builtin_amdgcn_mov_dpp(y1, 0x4E, 0xF, 0xF, true);
  const unsigned d2 = __builtin_amdgcn_mov_dpp(y2, 0x4E, 0xF, 0xF, true), d3 = __builtin_amdgcn_mov_dpp(y3, 0x4E, 0xF, 0xF, true);
  return v4u{q ? d2 : y0, q ? d3 : y1, q ? y2 : d0, q ? y3 : d1};
}
DEV c2 cj(c2 a) { return {a.re, -a.im}; }
DEV c2 cmulj(c2 a, c2 b) { return {fma2(a.re, b.re, -(a.im * b.im)), fma2(-a.re, b.im, -(a.im * b.re))}; }

__global__ __launch_bounds__(512, 2) void fused_ssm(
    const u16* __restrict__ x, const u16* __restrict__ Lre, const u16* __restrict__ Lim, const u16* __restrict__ Bre,
    const u16* __restrict__ Bim, const u16* __restrict__ Cre, const u16* __restrict__ Cim, const u16* __restrict__ ldtA,
    const u16* __restrict__ DA, const u16* __restrict__ u, const u16* __restrict__ lA, const u16* __restrict__ Aim,
    const u16* __restrict__ Cr, const u16* __restrict__ Ci, const u16* __restrict__ ldtB, const u16* __restrict__ DB,
    float* out, char* ws) {
#if defined(__HIP_DEVICE_COMPILE__)
  __shared__ __attribute__((aligned(16))) char smem_[163840];
  LAS char* smem = (LAS char*)smem_;
  u64 stamp[16] = {};
  u64 pc0;
  asm volatile("s_getpc_b64 %0" : "=s"(pc0));
  const int b = blockIdx.x, xcd = b & 7, j = b >> 3;
  const int tid = threadIdx.x, lane = tid & 63, w = __builtin_amdgcn_readfirstlane(tid >> 6);
  const int r = lane & 15, g = lane >> 4;
  STAMP(0);

  if (__builtin_expect(j >= 18, 0)) {
  if (j < 27) {
    const int d0 = 72 * xcd + 8 * (j - 18), d = d0 + w;
    LAS char* sc = smem + w * 20464;
    const int tb = ((tid >> 1) + 32 * (j - 18)) & 255, hf = tid & 1;
    const unsigned qa = *(const GAS unsigned*)(lA + d * 128 + 2 * lane), qi = *(const GAS unsigned*)(Aim + d * 128 + 2 * lane);
    const unsigned qr = *(const GAS unsigned*)(Cr + d * 128 + 2 * lane), qc = *(const GAS unsigned*)(Ci + d * 128 + 2 * lane);
    const v2u dk = *(const GAS v2u*)(DB + d0 + 4 * hf);
    const float dt = rbf(__builtin_amdgcn_exp2f(bflo(ldtB[d]) * LOG2E));
    const f32x2 l2 = bf2(pk2(exp2v(bf2(qa) * LOG2E))) * (-dt) * LOG2E, rv = redrev(bf2(qi) * dt);
    FENCE();
    STAMP(1);
    v2u uv[8];
    const auto rsU = __builtin_amdgcn_make_buffer_rsrc((void*)u, 0, 2048 * 1152, 0x00020000);
    const unsigned uvo = (8 * tb) * 1152 + (d0 + 4 * hf) * 2;
#define ULOAD(i) do { FENCE(); uv[i] = __builtin_amdgcn_raw_buffer_load_b64(rsU, uvo, (i) * 1152, 0); FENCE(); } while (0)
    ULOAD(0); ULOAD(1);
    f32x4 kacc[3] = {};
    {
      const c2 w1 = cpow(l2, rv, sp(1.f)), v8 = cpow(l2, -rv, sp(8.f)), Q = cpow(l2, rv, sp(128.f));
      LAS char* tw = sc + lane * 8;
      LAS char* tr = sc + r * 528 + g * 16;
#define PUTG(row, z) (*(LAS v2u*)(tw + (row) * 528) = v2u{pk(z.re[0], z.im[0]), pk(z.re[1], z.im[1])})
      *(LAS v2u*)(tw) = v2u{0x3F80u, 0x3F80u};
      c2 f = v8;
      PUTG(1, f);
#pragma unroll
      for (int i = 2; i < 16; ++i) { f = cmul(f, v8); PUTG(i, f); if (i == 4) ULOAD(2); if (i == 8) ULOAD(3); if (i == 12) ULOAD(4); }
      ULOAD(5);
      v4u fF[8];
#pragma unroll
      for (int ks = 0; ks < 8; ++ks) fF[ks] = lds128(tr + ks * 64);
      c2 R[8];
      R[0] = {bf2(qr) * (2.f * dt), bf2(qc) * (2.f * dt)};
#pragma unroll
      for (int i = 1; i < 8; ++i) R[i] = cmul(R[i - 1], w1);
      ULOAD(6);
#pragma unroll
      for (int i = 0; i < 8; ++i) { PUTG(i, R[i]); R[i] = cmul(R[i], Q); PUTG(8 + i, R[i]); if (i == 2) ULOAD(7); }
#pragma unroll
      for (int ks = 0; ks < 8; ++ks) kacc[0] = mfma(lds128(tr + ks * 64), fF[ks], kacc[0]);
#pragma unroll
      for (int cc = 0; cc < 4; ++cc) {
        const unsigned sel = (cc & 1) ? 0x07060302u : 0x05040100u;
        v4u o;
#pragma unroll
        for (int k = 0; k < 4; ++k) o[k] = perm(uv[2 * k + 1][cc >> 1], uv[2 * k][cc >> 1], sel);
        LAS char* ub = smem + (4 * hf + cc) * 20464 + 8448;
        *(LAS v4u*)(ub + 4640 + (8 * tb + 224) * 2) = o;
        LAS unsigned* u0 = (LAS unsigned*)(ub + (8 * tb + 226) * 2);
        u0[0] = o[0]; u0[1] = o[1]; u0[2] = o[2]; u0[3] = o[3];
      }
      STAMP(2);
      BARRIER();
      if (w < 4) __builtin_amdgcn_s_setprio(1);
#pragma unroll
      for (int s = 0; s < 2; ++s)
#pragma unroll
        for (int i = 0; i < 4; ++i) { R[2 * i] = cmul(R[2 * i], Q); PUTG(4 * s + i, R[2 * i]); }
#pragma unroll
      for (int s = 0; s < 4; ++s)
#pragma unroll
        for (int i = 0; i < 2; ++i) { R[4 * i] = cmul(R[4 * i], Q); PUTG(8 + 2 * s + i, R[4 * i]); }
#pragma unroll
      for (int ks = 0; ks < 8; ++ks) kacc[1] = mfma(lds128(tr + ks * 64), fF[ks], kacc[1]);
#pragma unroll
      for (int s = 0; s < 9; ++s) { R[0] = cmul(R[0], Q); PUTG(s, R[0]); }
#pragma unroll
      for (int ks = 0; ks < 8; ++ks) kacc[2] = mfma(lds128(tr + ks * 64), fF[ks], kacc[2]);
    }
    FENCE();
    STAMP(3);
    {
      LAS char* tt = sc + 17728;
      const unsigned aA = g < 2 ? (4 * r + 64 * g) * 2 : (128 + 2 * r + 64 * (g - 2)) * 2;
      *(LAS unsigned*)(tt + aA) = pk(kacc[1][0], kacc[1][1]);
      *(LAS unsigned*)(tt + aA + (g < 2 ? 4 : 64)) = pk(kacc[1][2], kacc[1][3]);
      const unsigned k01 = pk(kacc[2][0], kacc[2][1]), k23 = pk(kacc[2][2], kacc[2][3]);
      LAS char* t3 = tt + (256 + 64 * g + r) * 2;
      *(LAS u16*)(t3) = (u16)k01; *(LAS u16*)(t3 + 32) = (u16)(k01 >> 16);
      *(LAS u16*)(t3 + 64) = (u16)k23; *(LAS u16*)(t3 + 96) = (u16)(k23 >> 16);
      if (lane < 17) *(LAS unsigned*)(sc + (1023 + lane) * 4) = 0u;
      if (lane < 16) *(LAS unsigned*)(sc + 4224 + (1024 + lane) * 4) = 0u;
      if (lane < 28) { *(LAS v4u*)(sc + 8448 + lane * 16) = v4u{0, 0, 0, 0}; *(LAS v4u*)(sc + 8448 + 4640 + lane * 16) = v4u{0, 0, 0, 0}; }
      if (lane == 28) *(LAS unsigned*)(sc + 8448 + 448) = 0u;
      FENCE();
      const unsigned a1 = *(LAS unsigned*)(tt + lane * 4), b1 = *(LAS unsigned*)(tt + lane * 4 + 4);
      const unsigned a2 = *(LAS unsigned*)(tt + 256 + lane * 4), b2 = *(LAS unsigned*)(tt + 256 + lane * 4 + 4);
      const unsigned a3 = *(LAS unsigned*)(tt + 512 + lane * 4), b3 = *(LAS unsigned*)(tt + 512 + lane * 4 + 4);
      {
        const unsigned e0 = perm(a1, a1, 0x01000100u), m10 = perm(a1, a1, 0x01000302u), m21 = perm(a1, b1, 0x07060100u);
        *(LAS v2u*)(sc + 4224 + (1788 - 4 * lane) * 2) = v2u{m21, e0};
        *(LAS v2u*)(sc + (1788 - 4 * lane) * 2) = v2u{m10, e0};
      }
      {
        const unsigned e0 = perm(a2, a2, 0x01000100u), e1 = perm(a2, a2, 0x03020302u), e2 = perm(b2, b2, 0x01000100u);
        const unsigned m10 = perm(a2, a2, 0x01000302u), m21 = perm(a2, b2, 0x07060100u);
        *(LAS v4u*)(sc + 4224 + (1528 - 8 * lane) * 2) = v4u{e2, e1, m10, e0};
        *(LAS v4u*)(sc + (1528 - 8 * lane) * 2) = v4u{m21, e1, e0, e0};
      }
      {
        const unsigned e0 = perm(a3, a3, 0x01000100u), e1 = perm(a3, a3, 0x03020302u), e2 = perm(b3, b3, 0x01000100u);
        const unsigned m10 = perm(a3, a3, 0x01000302u), m21 = perm(a3, b3, 0x07060100u);
        LAS char* p1 = sc + 4224 + (1008 - 16 * lane) * 2;
        LAS char* p0_ = sc + (1008 - 16 * lane) * 2;
        *(LAS v4u*)(p1) = v4u{e2, e2, e1, e1}; *(LAS v4u*)(p1 + 16) = v4u{e1, m10, e0, e0};
        *(LAS v4u*)(p0_) = v4u{e2, m21, e1, e1}; *(LAS v4u*)(p0_ + 16) = v4u{e1, e0, e0, e0};
      }
      FENCE();
      {
        const int q0 = 8 * r + 4 * (g & 1) + 128 * (g >> 1);
        const unsigned v32 = pk(kacc[0][3], kacc[0][2]), v10 = pk(kacc[0][1], kacc[0][0]), v21 = pk(kacc[0][2], kacc[0][1]);
        *(LAS v2u*)(sc + 4224 + (2044 - q0) * 2) = v2u{v32, v10};
        *(LAS u16*)(sc + (2043 - q0) * 2) = (u16)v32;
        *(LAS unsigned*)(sc + (2044 - q0) * 2) = v21;
        *(LAS u16*)(sc + (2046 - q0) * 2) = (u16)(v10 >> 16);
      }
    }
    FENCE();
    STAMP(4);
    {
      f32x4 ya[8] = {};
      LAS char* pT = sc + (r & 1) * 4224 + 504 - 8 * (r >> 1) + 16 * g;
      LAS char* pT2 = pT + 8;
      asm volatile("" : "+v"(pT2));
      LAS char* pU = sc + 8448 + (r & 1) * 4640 + 448 + 64 * (r >> 1) + 16 * g;
      v4u U[8], Tq[2];
#define LDT(off) ({ const v2u a_ = lds64(pT + (off)), b_ = lds64(pT2 + (off)); v4u{a_[0], a_[1], b_[0], b_[1]}; })
      Tq[0] = LDT(512 * 7);
#pragma unroll
      for (int k = 7; k >= 0; --k) U[k] = lds128(pU + 512 * k);
      Tq[1] = LDT(512 * 6);
#pragma unroll 1
      for (int rr = 0; rr < 8; ++rr) {
#pragma unroll
        for (int m = 0; m < 8; ++m) {
          const v4u T = Tq[m & 1];
          Tq[m & 1] = m < 6 ? LDT(512 * (5 - m)) : LDT(512 * (13 - m) - 64);
#pragma unroll
          for (int k = 7 - m; k >= 0; --k) ya[k + m] = mfma(T, U[k], ya[k + m]);
          U[7 - m] = lds128(pU + 512 * (7 - m) - 64);
          SCHED();
        }
        pT -= 64; pT2 -= 64; pU -= 64;
      }
      FENCE();
      LAS char* py = sc + (4 * g + (r & 1) + 16 * (r >> 1)) * 4;
#pragma unroll
      for (int q = 0; q < 8; ++q) {
        *(LAS unsigned*)(py + 512 * q) = pk(ya[q][0], ya[q][1]);
        *(LAS unsigned*)(py + 512 * q + 8) = pk(ya[q][2], ya[q][3]);
      }
    }
    STAMP(5);
    BARRIER();
    {
      v4u yv[4];
#pragma unroll
      for (int cc = 0; cc < 4; ++cc) yv[cc] = lds128(smem + (4 * hf + cc) * 20464 + 16 * tb);
      const auto rsO = __builtin_amdgcn_make_buffer_rsrc(out, 0, 2048 * 1152 * 4, 0x00020000);
      const unsigned op = ((8 * tb) * 1152 + 576 + d0 + 4 * hf) * 4;
      const f32x2 d01 = bf2(dk[0]), d23 = bf2(dk[1]);
#pragma unroll
      for (int i = 0; i < 8; ++i) {
        const f32x2 s01 = bf2(pk2(bf2(uv[i][0]) * d01)), s23 = bf2(pk2(bf2(uv[i][1]) * d23));
        f32x2 y01, y23;
        if (i & 1) { y01 = f32x2{bfhi(yv[0][i >> 1]), bfhi(yv[1][i >> 1])}; y23 = f32x2{bfhi(yv[2][i >> 1]), bfhi(yv[3][i >> 1])}; }
        else { y01 = f32x2{bflo(yv[0][i >> 1]), bflo(yv[1][i >> 1])}; y23 = f32x2{bflo(yv[2][i >> 1]), bflo(yv[3][i >> 1])}; }
        const f32x2 o01 = bf2(pk2(y01 + s01)), o23 = bf2(pk2(y23 + s23));
        __builtin_amdgcn_raw_buffer_store_b128(__builtin_bit_cast(v4u, f32x4{o01[0], o01[1], o23[0], o23[1]}), rsO, op + i * 4608, 0, AUXB);
      }
    }
    STAMP(15);
    WAITV(0);
    STAMP(14);
    STAMP_FLUSH();
  } else if (b != 255) {
    unsigned v = 0;
    if (j == 27 && tid < CODE_LINES) v = *(const GAS unsigned*)((pc0 & ~127ull) + tid * 128);
    if (j < 31) {
      const int R = ((j - 27) * 8 + w) * 36 + (lane < 36 ? lane : 35);
      const u64 bx = (u64)x + (xcd >> 1) * 512 * 1152, br = (u64)Bre + ((xcd & 1) * 256 - 512) * 1152, bi = (u64)Bim + ((xcd & 1) * 256 - 832) * 1152;
      const u64 base = (R < 512 ? bx : (R < 832 ? br : bi)) + R * 1152;
      unsigned t[9];
#pragma unroll
      for (int k = 0; k < 9; ++k) t[k] = *(const GAS unsigned*)(base + k * 128);
#pragma unroll
      for (int k = 0; k < 9; ++k) v ^= t[k];
    }
    asm volatile("" ::"v"(v));
  } else if (w == 0) {
    const u64 tag = *(const CAS u64*)ws + TAG_STEP;
    const GAS u64* fl = (const GAS u64*)(ws + WS_FLAG);
#pragma unroll 1
    for (unsigned sp_ = 0; sp_ < SPIN_CAP; ++sp_) {
      __builtin_amdgcn_s_sleep(16);
      FENCE();
      const u64 f0 = __hip_atomic_load(fl + lane * 16, __ATOMIC_RELAXED, __HIP_MEMORY_SCOPE_AGENT);
      const u64 f1 = __hip_atomic_load(fl + (64 + lane) * 16, __ATOMIC_RELAXED, __HIP_MEMORY_SCOPE_AGENT);
      const u64 f2 = __hip_atomic_load(fl + (128 + (lane & 15)) * 16, __ATOMIC_RELAXED, __HIP_MEMORY_SCOPE_AGENT);
      if (__all(f0 == tag && f1 == tag && f2 == tag)) break;
    }
    {
      u64 mn = ~0ull, mx = 0;
      for (int i = 0; i < 4; ++i) {
        const int bb = i * 64 + lane;
        if (bb < 216) {
          const GAS u64* sp = (const GAS u64*)(ws + WS_DBG) + bb * 16;
          for (unsigned sp_ = 0; sp_ < SPIN_CAP; ++sp_) { if (__hip_atomic_load(sp + 2, __ATOMIC_RELAXED, __HIP_MEMORY_SCOPE_AGENT) == tag) break; __builtin_amdgcn_s_sleep(8); }
          const u64 a = __hip_atomic_load(sp + 0, __ATOMIC_RELAXED, __HIP_MEMORY_SCOPE_AGENT), e = __hip_atomic_load(sp + 1, __ATOMIC_RELAXED, __HIP_MEMORY_SCOPE_AGENT);
          mn = a < mn ? a : mn;
          if ((bb < 144) == (AMP_ROLE == 0)) mx = e > mx ? e : mx;
        }
      }
      for (int o = 1; o < 64; o <<= 1) { const u64 a = __shfl_xor(mn, o), e = __shfl_xor(mx, o); mn = a < mn ? a : mn; mx = e > mx ? e : mx; }
      u64 v = mx - mn; v = v > 10000 ? 10000 : v;
      const u64 until = __builtin_amdgcn_s_memrealtime() + AMP_F * v + AMP_FIX;
      while (__builtin_amdgcn_s_memrealtime() < until) __builtin_amdgcn_s_sleep(4);
    }
    if (lane == 0) __hip_atomic_store((GAS u64*)ws, tag, __ATOMIC_RELAXED, __HIP_MEMORY_SCOPE_AGENT);
  }
  return;
  }
  {
    const int odd = xcd & 1, mid = j >> 4;
    const int pi = mid ? (j - 16) + 2 * odd : (j >> 2), gi = mid ? 4 : (j & 3);
    const int pg = mid ? 4 : gi + 5 * odd, c = 4 * (xcd >> 1) + pi;
    const int wm = w & 3, wn = w >> 2;
    const int p0 = 64 * pg + 32 * wn + 2 * r;
    const unsigned pLr = *(const GAS unsigned*)(Lre + p0), pLi = *(const GAS unsigned*)(Lim + p0), pDt = *(const GAS unsigned*)(ldtA + p0);
    FENCE();
    const u64 sx = (u64)x + (c * 128 + w * 32) * 1152, sb = (u64)(w < 6 ? Bre : Bim) + (pg * 64 + (w & 1) * 32) * 1152;
    const auto rsS = __builtin_amdgcn_make_buffer_rsrc((void*)(wn ? sb : sx), 0, 32 * 1152, 0x00020000);
    const int l3 = lane >> 3, ch = (lane & 7) ^ (l3 >> 1);
    const int rowx = 8 * (l3 >> 2) + 4 * (l3 & 1) + ((l3 >> 1) & 1), rowb = rowx ^ ((rowx ^ (2 * l3)) & -wn);
    const unsigned dvA = __umul24(rowb, 1152u) + ch * 16, dvB = dvA + 16 * 1152 + 64 - 2 * (dvA & 64);
    const int stp = (2 - wn) * 1152;
#define SLOT(s) (((8 - (s)) % 3) * 32768)
#define SPIECE(s, i) __builtin_amdgcn_raw_ptr_buffer_load_lds(rsS, smem + SLOT(s) + w * 4096 + (i) * 1024, 16, ((i) & 1) ? dvB : dvA, (s) * 128 + ((i) >> 1) * stp, 0, 0)
    SPIECE(0, 0); SPIECE(0, 1); SPIECE(0, 2); SPIECE(0, 3);
    asm volatile("s_barrier" ::: "memory");
    SPIECE(1, 0); SPIECE(1, 1); SPIECE(1, 2); SPIECE(1, 3);
    FENCE();
    const u64 tag = *(const CAS u64*)ws + TAG_STEP;
    const unsigned tagA = (unsigned)tag, tagB = (unsigned)(tag >> 32);
    const auto rsW = __builtin_amdgcn_make_buffer_rsrc(ws, 0, WS_END, 0x00020000);
    const int c2_ = 2 * xcd + (j >= 9), n = j >= 9 ? j - 9 : j;
    const auto rsCre = __builtin_amdgcn_make_buffer_rsrc((void*)(Cre + 64 * n * 576), 0, 64 * 1152, 0x00020000);
    const auto rsCim = __builtin_amdgcn_make_buffer_rsrc((void*)(Cim + 64 * n * 576), 0, 64 * 1152, 0x00020000);
    const unsigned cvo = __umul24(32 * (w & 1) + 4 * l3 + (w >> 1), 1152u) + (((lane & 7) ^ ((4 * w + (l3 >> 1)) & 7)) * 16);
#define CPIECE(q) __builtin_amdgcn_raw_ptr_buffer_load_lds((q) < 9 ? rsCre : rsCim, smem + 16384 + (q) * 8192 + w * 1024, 16, cvo, ((q) % 9) * 128, 0, 0)

    const f32x2 lr = bf2(pLr), li = bf2(pLi), dtA = bf2(pk2(exp2v(bf2(pDt) * LOG2E)));
    c2 lam = cpow(dtA * lr * LOG2E, redrev(dtA * li), sp(1.f));
    c2 l4 = csqr(csqr(lam)), l8 = csqr(l4), l16 = csqr(l8), l32 = csqr(l16);
    asm volatile("" : "+v"(lam.re), "+v"(lam.im), "+v"(l4.re), "+v"(l4.im), "+v"(l8.re), "+v"(l8.im), "+v"(l16.re), "+v"(l16.im), "+v"(l32.re), "+v"(l32.im));

    const unsigned fo = r * 128 + ((g ^ (r >> 1)) * 16);
    LAS char* pa0 = smem + wm * 4096 + fo;
    LAS char* pa1 = smem + wm * 4096 + (fo ^ 64);
    LAS char* pb0 = smem + 16384 + wn * 4096 + fo;
    LAS char* pb1 = smem + 16384 + wn * 4096 + (fo ^ 64);
    f32x4 acc[2][2][2] = {};
    v4u fr[2][12];
    STAMP(1);
#pragma unroll
    for (int s = 0; s < 10; ++s) {
      if (s <= 6) WAITV(4); else if (s <= 8) WAITV(8);
      if (s == 1) asm volatile("" ::"s"(tagA), "s"(tagB));
      BARRIER();
      if (s == 0) STAMP(2);
      if (s == 3) STAMP(11);
      if (s == 6) STAMP(12);
      if (s == 9) STAMP(13);
#pragma unroll
      for (int i = 0; i < 16; ++i) {
        if (s < 9 && i < 12) {
          const int kk = i / 6, e = i % 6;
          LAS char* pa = (kk ? pa1 : pa0) + SLOT(s);
          LAS char* pb = (kk ? pb1 : pb0) + SLOT(s);
          fr[s & 1][i] = e == 0 ? lds128(pa) : e == 5 ? lds128(pa + 2048) : lds128(pb + ((e - 1) >> 1) * 8192 + ((e - 1) & 1) * 2048);
        }
        if (s >= 1) {
          const int kk = i >> 3, m = (i >> 2) & 1, e = i & 3;
          acc[m][e >> 1][e & 1] = mfma(fr[(s - 1) & 1][6 * kk + 5 * m], fr[(s - 1) & 1][6 * kk + 1 + e], acc[m][e >> 1][e & 1]);
        }
        if ((i & 3) == 3) {
          if (s + 2 < 9) SPIECE(s + 2, i >> 2);
          if (s == 7) CPIECE(14 + (i >> 2));
          if (s == 8) CPIECE(6 + (i >> 2));
        }
        SCHED();
      }
      if (s == 6) { CPIECE(10); CPIECE(11); CPIECE(12); CPIECE(13); SCHED(); }
    }
    STAMP(3);
#define BSTEP(t, s) c2{f32x2{acc[(s) >> 1][0][t][2 * ((s) & 1)], acc[(s) >> 1][0][t][2 * ((s) & 1) + 1]}, f32x2{acc[(s) >> 1][1][t][2 * ((s) & 1)], acc[(s) >> 1][1][t][2 * ((s) & 1) + 1]}}
    c2 eA, ea, ea2;
    LAS char* sW = smem;
    LAS char* sH = smem + 2048;
    {
      c2 e2[2];
#pragma unroll
      for (int t = 0; t < 2; ++t) {
        e2[t] = BSTEP(t, 0);
#pragma unroll
        for (int s = 1; s < 4; ++s) e2[t] = cfma(el(lam, t), e2[t], BSTEP(t, s));
      }
      eA = {f32x2{e2[0].re[0], e2[1].re[0]}, f32x2{e2[0].im[0], e2[1].im[0]}};
      const c2 e = cfma(l4, eA, c2{f32x2{e2[0].re[1], e2[1].re[1]}, f32x2{e2[0].im[1], e2[1].im[1]}});
      c2 hi;
      ea.re = swap16(e.re, hi.re); ea.im = swap16(e.im, hi.im);
      const c2 A = cfma(l8, ea, hi);
      ea2.re = swap32(A.re, hi.re); ea2.im = swap32(A.im, hi.im);
      const c2 W = cfma(l16, ea2, hi);
      if (g == 0) *(LAS f32x4*)(sW + (wm * 32 + 16 * wn + r) * 16) = f32x4{W.re[0], W.re[1], W.im[0], W.im[1]};
    }
    const bool pub = w == 0 || w == 5;
    if (pub) __builtin_amdgcn_s_setprio(2);
    BARRIER();
    c2 pw;
    {
      f32x4 wv[4];
#pragma unroll
      for (int i = 0; i < 4; ++i) wv[i] = *(LAS f32x4*)(sW + (i * 32 + 16 * wn + r) * 16);
      const c2 s1 = {wv[0].xy, wv[0].zw};
      const c2 s2 = cfma(l32, s1, c2{wv[1].xy, wv[1].zw});
      const c2 s3 = cfma(l32, s2, c2{wv[2].xy, wv[2].zw});
      if (pub) {
        const c2 E = cfma(l32, s3, c2{wv[3].xy, wv[3].zw});
        if (g < 2)
          __builtin_amdgcn_raw_buffer_store_b128(v4u{__float_as_uint(g ? E.re[1] : E.re[0]), tagA, __float_as_uint(g ? E.im[1] : E.im[0]), tagB}, rsW,
                                                 WS_E + (c * 576 + p0 + g) * 16, 0, 16);
        __builtin_amdgcn_s_setprio(0);
      }
      pw.re = wm == 0 ? sp(0.f) : wm == 1 ? s1.re : wm == 2 ? s2.re : s3.re;
      pw.im = wm == 0 ? sp(0.f) : wm == 1 ? s1.im : wm == 2 ? s2.im : s3.im;
    }
    FENCE();
    STAMP(4);
    const unsigned qLr = Lre[64 * pg + lane], qLi = Lim[64 * pg + lane], qDt = ldtA[64 * pg + lane];
    FENCE();
    CPIECE(0); CPIECE(1); CPIECE(2); CPIECE(3); CPIECE(4); CPIECE(5);
    FENCE();
    const int t2 = 128 * c2_ + 32 * (w >> 1) + 4 * (w & 1) + 8 * g;
    v2u xs[4];
#pragma unroll
    for (int i = 0; i < 4; ++i) xs[i] = *(const GAS v2u*)(x + (t2 + i) * 576 + 64 * n + 4 * r);
    const v2u dsk = *(const GAS v2u*)(DA + 64 * n + 4 * r);
    FENCE();
    c2 pf2[2], lp2[2], bs[2][4];
    {
      const f32x2 den = lr * lr + li * li, inv = f32x2{__builtin_amdgcn_rcpf(den[0]), __builtin_amdgcn_rcpf(den[1])} * 2.f, nr = lam.re - 1.f;
      const c2 coef = {(nr * lr + lam.im * li) * inv, (lam.im * lr - nr * li) * inv};
      const c2 z = {sp(0.f), sp(0.f)}, one = {sp(1.f), sp(0.f)};
      const c2 lg = cmul((g & 1) ? l8 : one, (g & 2) ? l16 : one);
      const c2 lwv = cmul((wm & 1) ? l32 : one, (wm & 2) ? csqr(l32) : one);
      const c2 lw = cmul(cmul(coef, lg), lwv);
      const c2 o = (g & 2) ? ea2 : z;
      const c2 pin = (g & 1) ? cfma(l8, o, ea) : o;
      const c2 pfA = cmul(coef, cfma(lg, pw, pin));
      const c2 pfB = cfma(l4, pfA, cmul(coef, eA)), lwB = cmul(l4, lw);
#pragma unroll
      for (int t = 0; t < 2; ++t) {
        pf2[t] = {f32x2{pfA.re[t], pfB.re[t]}, f32x2{-pfA.im[t], -pfB.im[t]}};
        lp2[t] = {f32x2{lw.re[t], lwB.re[t]}, f32x2{-lw.im[t], -lwB.im[t]}};
#pragma unroll
        for (int s = 0; s < 4; ++s) {
          bs[t][s] = cmulj(el(coef, t), BSTEP(t, s));
          asm volatile("" : "+v"(bs[t][s].re), "+v"(bs[t][s].im));
        }
        asm volatile("" : "+v"(pf2[t].re), "+v"(pf2[t].im), "+v"(lp2[t].re), "+v"(lp2[t].im));
      }
    }
    if (w < 4) {
      float hr = 0.f, hi = 0.f;
      if (w < c) {
        const float dt = rbf(__builtin_amdgcn_exp2f(bflo(qDt) * LOG2E));
        const f32x2 ql2 = sp(dt * bflo(qLr) * LOG2E), qrv = redrev(sp(dt * bflo(qLi)));
        const int n0 = c - 1 - w;
        const c2 wa = cpow(ql2, qrv, f32x2{(float)(128 * n0), (float)(128 * (n0 > 4 ? n0 - 4 : 0))});
        const c2 wb = cpow(ql2, qrv, f32x2{(float)(128 * (n0 > 8 ? n0 - 8 : 0)), (float)(128 * (n0 > 12 ? n0 - 12 : 0))});
        const unsigned eo = WS_E + (w * 576 + 64 * pg + lane) * 16;
        const unsigned o1 = n0 >= 4 ? 4 * 9216 : 0, o2 = n0 >= 8 ? 8 * 9216 : 0, o3 = n0 >= 12 ? 12 * 9216 : 0;
        const f32x2 ma = {1.f, n0 >= 4 ? 1.f : 0.f}, mb = {n0 >= 8 ? 1.f : 0.f, n0 >= 12 ? 1.f : 0.f};
        c2 wa2 = {wa.re * ma, wa.im * ma}, wb2 = {wb.re * mb, wb.im * mb};
        asm volatile("" : "+v"(wa2.re), "+v"(wa2.im), "+v"(wb2.re), "+v"(wb2.im));
        v4u e0, e1, e2, e3;
#pragma unroll 1
        for (unsigned sp_ = 0; sp_ < SPIN_CAP; ++sp_) {
          FENCE();
          e0 = __builtin_amdgcn_raw_buffer_load_b128(rsW, eo, 0, 16);
          e1 = __builtin_amdgcn_raw_buffer_load_b128(rsW, eo, o1, 16);
          e2 = __builtin_amdgcn_raw_buffer_load_b128(rsW, eo, o2, 16);
          e3 = __builtin_amdgcn_raw_buffer_load_b128(rsW, eo, o3, 16);
          const unsigned bad = ((e0[1] ^ tagA) | (e0[3] ^ tagB) | (e1[1] ^ tagA)) | ((e1[3] ^ tagB) | (e2[1] ^ tagA) | (e2[3] ^ tagB)) | ((e3[1] ^ tagA) | (e3[3] ^ tagB));
          if (__all(bad == 0)) break;
          __builtin_amdgcn_s_sleep(1);
        }
        const c2 hp = cfma(wa2, c2{f32x2{__uint_as_float(e0[0]), __uint_as_float(e1[0])}, f32x2{__uint_as_float(e0[2]), __uint_as_float(e1[2])}},
                           cmul(wb2, c2{f32x2{__uint_as_float(e2[0]), __uint_as_float(e3[0])}, f32x2{__uint_as_float(e2[2]), __uint_as_float(e3[2])}}));
        hr = hp.re[0] + hp.re[1]; hi = hp.im[0] + hp.im[1];
      }
      LAS float* hq = (LAS float*)(sH + (w * 32 + (lane >> 1)) * 16) + (lane & 1);
      hq[0] = hr; hq[2] = -hi;
    }
    STAMP(5);
    BARRIER();
    {
      const f32x4 hs = (*(LAS f32x4*)(sH + (16 * wn + r) * 16) + *(LAS f32x4*)(sH + (32 + 16 * wn + r) * 16)) +
                       (*(LAS f32x4*)(sH + (64 + 16 * wn + r) * 16) + *(LAS f32x4*)(sH + (96 + 16 * wn + r) * 16));
      const c2 H = {hs.xy, hs.zw}, lamj = cj(lam);
      c2 h0 = cfma(lp2[0], el(H, 0), pf2[0]), h1 = cfma(lp2[1], el(H, 1), pf2[1]);
      unsigned dv[4][4];
#pragma unroll
      for (int s = 0; s < 4; ++s) {
        h0 = cfma(el(lamj, 0), h0, bs[0][s]);
        h1 = cfma(el(lamj, 1), h1, bs[1][s]);
        dv[0][s] = pk(h0.re[0], h1.re[0]); dv[2][s] = pk(h0.re[1], h1.re[1]);
        dv[1][s] = pk(h0.im[0], h1.im[0]); dv[3][s] = pk(h0.im[1], h1.im[1]);
      }
      LAS char* tb_ = smem + 8192 + w * 1024;
      LAS char* tw_ = tb_ + ((r >> 2) * 16 + 4 * g) * 16 + (r & 3) * 4;
      LAS char* tr_ = tb_ + (lane ^ g) * 16;
      const unsigned hoff = WS_H + (4 * c + wm) * 2 * 36864 + (2 * pg + wn) * 2048 + lane * 16;
#pragma unroll
      for (int e = 0; e < 4; ++e) {
#pragma unroll
        for (int s = 0; s < 4; ++s) *(LAS unsigned*)(tw_ + ((s ^ (r >> 2)) * 16)) = dv[e][s];
        FENCE();
        const v4u o = lds128(tr_);
        FENCE();
        __builtin_amdgcn_raw_buffer_store_b128(o, rsW, hoff + (e >> 1) * 36864 + (e & 1) * 1024, 0, 16);
      }
    }
    STAMP(6);
    WAITV(0);
    BARRIER();
    STAMP(7);
    if (w == 0) {
      if (lane == 0) __hip_atomic_store((GAS u64*)(ws + WS_FLAG) + (c * 9 + pg) * 16, tag, __ATOMIC_RELAXED, __HIP_MEMORY_SCOPE_AGENT);
      const GAS u64* fp = (const GAS u64*)(ws + WS_FLAG) + (c2_ * 9 + (lane < 9 ? lane : 0)) * 16;
#pragma unroll 1
      for (unsigned sp_ = 0; sp_ < SPIN_CAP; ++sp_) {
        FENCE();
        if (__all(__hip_atomic_load(fp, __ATOMIC_RELAXED, __HIP_MEMORY_SCOPE_AGENT) == tag)) break;
        __builtin_amdgcn_s_sleep(1);
      }
    }
    BARRIER();
    STAMP(8);
    {
      constexpr int HD = 8;
      const unsigned hsrc = WS_H + (c2_ * 8 + w) * 36864;
      v4u hf[HD];
#pragma unroll
      for (int i = 0; i < HD; ++i) hf[i] = __builtin_amdgcn_raw_buffer_load_b128(rsW, lane * 16, hsrc + i * 1024, 16);
      LAS char* pc0_ = smem + 16384 + fo;
      LAS char* pc1_ = smem + 16384 + (fo ^ 64);
      f32x4 a[4] = {};
      v4u cf[2][4];
#define CFRAG(k1, jt) lds128(((((k1) >> 1) & 1) ? pc1_ : pc0_) + (((k1) & 1) * 9 + ((k1) >> 2)) * 8192 + (jt) * 2048)
#pragma unroll
      for (int jt = 0; jt < 4; ++jt) cf[0][jt] = CFRAG(0, jt);
#pragma unroll
      for (int ks = 0; ks < 36; ++ks) {
        const v4u hc = hf[ks % HD];
#pragma unroll
        for (int jt = 0; jt < 4; ++jt) {
          if (ks + 1 < 36) cf[(ks + 1) & 1][jt] = CFRAG(ks + 1, jt);
          a[jt] = mfma(hc, cf[ks & 1][jt], a[jt]);
          SCHED();
        }
        if (ks + HD < 36) hf[ks % HD] = __builtin_amdgcn_raw_buffer_load_b128(rsW, lane * 16, hsrc + (ks + HD) * 1024, 16);
        SCHED();
        if (ks == 0) STAMP(9);
      }
      STAMP(10);
      const auto rsO = __builtin_amdgcn_make_buffer_rsrc(out, 0, 2048 * 1152 * 4, 0x00020000);
      const f32x2 d01 = bf2(dsk[0]), d23 = bf2(dsk[1]);
#pragma unroll
      for (int i = 0; i < 4; ++i) {
        const f32x2 s01 = bf2(pk2(bf2(xs[i][0]) * d01)), s23 = bf2(pk2(bf2(xs[i][1]) * d23));
        __builtin_amdgcn_raw_buffer_store_b128(__builtin_bit_cast(v4u, f32x4{a[0][i] + s01[0], a[1][i] + s01[1], a[2][i] + s23[0], a[3][i] + s23[1]}), rsO,
                                               ((t2 + i) * 1152 + 64 * n + 4 * r) * 4, 0, AUXA);
      }
    }
    STAMP(15);
    WAITV(0);
    STAMP(14);
    STAMP_FLUSH();
  }
#endif
}

extern "C" void kernel_launch(void* const* d_in, const int* in_sizes, int n_in, void* d_out, int out_size, void* d_ws,
                              size_t ws_size, hipStream_t stream) {
  fused_ssm<<<256, 512, 0, stream>>>((const u16*)d_in[0], (const u16*)d_in[1], (const u16*)d_in[2], (const u16*)d_in[3],
                                     (const u16*)d_in[4], (const u16*)d_in[5], (const u16*)d_in[6], (const u16*)d_in[7],
                                     (const u16*)d_in[8], (const u16*)d_in[9], (const u16*)d_in[10], (const u16*)d_in[11],
                                     (const u16*)d_in[12], (const u16*)d_in[13], (const u16*)d_in[14], (const u16*)d_in[15],
                                     (float*)d_out, (char*)d_ws);
}
```

```cpp
#define AUXA 16
#include <hip/hip_runtime.h>
#include <stdint.h>

typedef unsigned short u16;
typedef unsigned long long u64;
typedef unsigned v4u __attribute__((ext_vector_type(4)));
typedef unsigned v2u __attribute__((ext_vector_type(2)));
typedef float f32x4 __attribute__((ext_vector_type(4)));
typedef float f32x2 __attribute__((ext_vector_type(2)));
typedef __bf16 bf16x8 __attribute__((ext_vector_type(8)));
typedef __bf16 bf16x2 __attribute__((ext_vector_type(2)));
#define CAS __attribute__((address_space(4)))
#define LAS __attribute__((address_space(3)))
#define GAS __attribute__((address_space(1)))
#define DEV __device__ __forceinline__

constexpr int WS_FLAG = 4096;
constexpr int WS_E = 65536;
constexpr int WS_H = 262144;
constexpr int WS_END = WS_H + 2048 * 1152 * 2;
constexpr int WS_DBG = 8 << 20;
constexpr unsigned SPIN_CAP = 400000u;
constexpr u64 TAG_STEP = 0x9E3779B97F4A7C15ull;
#define CODE_LINES 170

#define ROTATE 0
#define PREFETCH 1
#define AUXB 0
#define STAMP_ON 0
#define STAMP(i) do {} while (0)
#define STAMP_FLUSH() do {} while (0)

#define WAITV(n) asm volatile("s_waitcnt vmcnt(%0)" ::"n"(n) : "memory")
#define BARRIER() asm volatile("s_waitcnt lgkmcnt(0)\n\ts_barrier" ::: "memory")
#define FENCE() asm volatile("" ::: "memory")
#define SCHED() __builtin_amdgcn_sched_barrier(0)

struct c2 { f32x2 re, im; };
DEV f32x2 fma2(f32x2 a, f32x2 b, f32x2 c) { return __builtin_elementwise_fma(a, b, c); }
DEV f32x2 sp(float a) { return f32x2{a, a}; }
DEV c2 cmul(c2 a, c2 b) { return {fma2(a.re, b.re, -(a.im * b.im)), fma2(a.re, b.im, a.im * b.re)}; }
DEV c2 cfma(c2 a, c2 b, c2 c) { return {fma2(a.re, b.re, fma2(-a.im, b.im, c.re)), fma2(a.re, b.im, fma2(a.im, b.re, c.im))}; }
DEV c2 csqr(c2 a) { return {fma2(a.re, a.re, -(a.im * a.im)), (a.re + a.re) * a.im}; }
DEV c2 el(c2 a, int t) { return {sp(a.re[t]), sp(a.im[t])}; }
DEV float bflo(unsigned v) { return __uint_as_float(v << 16); }
DEV float bfhi(unsigned v) { return __uint_as_float(v & 0xFFFF0000u); }
DEV f32x2 bf2(unsigned v) { return f32x2{bflo(v), bfhi(v)}; }
DEV unsigned pk(float lo, float hi) { f32x2 v = {lo, hi}; return __builtin_bit_cast(unsigned, __builtin_convertvector(v, bf16x2)); }
DEV unsigned pk2(f32x2 v) { return __builtin_bit_cast(unsigned, __builtin_convertvector(v, bf16x2)); }
DEV float rbf(float f) { return bflo(pk(f, f)); }
constexpr float LOG2E = 1.44269504088896341f, INV2PI = 0.159154943091895336f;
DEV f32x2 exp2v(f32x2 x) { return f32x2{__builtin_amdgcn_exp2f(x[0]), __builtin_amdgcn_exp2f(x[1])}; }
DEV c2 cpow(f32x2 l2, f32x2 rev, f32x2 n) {
  const f32x2 m = exp2v(l2 * n), t0 = rev * n;
  const float ta = __builtin_amdgcn_fractf(t0[0]), tb = __builtin_amdgcn_fractf(t0[1]);
  return {m * f32x2{__builtin_amdgcn_cosf(ta), __builtin_amdgcn_cosf(tb)}, m * f32x2{__builtin_amdgcn_sinf(ta), __builtin_amdgcn_sinf(tb)}};
}
DEV f32x2 redrev(f32x2 ai) { const f32x2 r = ai * INV2PI; return r - f32x2{__builtin_rintf(r[0]), __builtin_rintf(r[1])}; }
DEV f32x4 mfma(v4u a, v4u b, f32x4 c) {
  return __builtin_amdgcn_mfma_f32_16x16x32_bf16(__builtin_bit_cast(bf16x8, a), __builtin_bit_cast(bf16x8, b), c, 0, 0, 0);
}
DEV v4u lds128(LAS char* p) { return *(LAS v4u*)p; }
DEV v2u lds64(LAS char* p) { return *(LAS v2u*)p; }
DEV unsigned perm(unsigned hi, unsigned lo, unsigned sel) { return __builtin_amdgcn_perm(hi, lo, sel); }
DEV f32x2 swap16(f32x2 v, f32x2& hi) {
  auto a = __builtin_amdgcn_permlane16_swap(__float_as_uint(v[0]), __float_as_uint(v[0]), false, false);
  auto b = __builtin_amdgcn_permlane16_swap(__float_as_uint(v[1]), __float_as_uint(v[1]), false, false);
  hi = f32x2{__uint_as_float(a[1]), __uint_as_float(b[1])};
  return f32x2{__uint_as_float(a[0]), __uint_as_float(b[0])};
}
DEV f32x2 swap32(f32x2 v, f32x2& hi) {
  auto a = __builtin_amdgcn_permlane32_swap(__float_as_uint(v[0]), __float_as_uint(v[0]), false, false);
  auto b = __builtin_amdgcn_permlane32_swap(__float_as_uint(v[1]), __float_as_uint(v[1]), false, false);
  hi = f32x2{__uint_as_float(a[1]), __uint_as_float(b[1])};
  return f32x2{__uint_as_float(a[0]), __uint_as_float(b[0])};
}
DEV v4u quadT(unsigned x0, unsigned x1, unsigned x2, unsigned x3, bool p, bool q) {
  const unsigned a0 = __builtin_amdgcn_mov_dpp(x0, 0xB1, 0xF, 0xF, true), a1 = __builtin_amdgcn_mov_dpp(x1, 0xB1, 0xF, 0xF, true);
  const unsigned a2 = __builtin_amdgcn_mov_dpp(x2, 0xB1, 0xF, 0xF, true), a3 = __builtin_amdgcn_mov_dpp(x3, 0xB1, 0xF, 0xF, true);
  const unsigned y0 = p ? a1 : x0, y1 = p ? x1 : a0, y2 = p ? a3 : x2, y3 = p ? x3 : a2;
  const unsigned d0 = __builtin_amdgcn_mov_dpp(y0, 0x4E, 0xF, 0xF, true), d1 = __builtin_amdgcn_mov_dpp(y1, 0x4E, 0xF, 0xF, true);
  const unsigned d2 = __builtin_amdgcn_mov_dpp(y2, 0x4E, 0xF, 0xF, true), d3 = __builtin_amdgcn_mov_dpp(y3, 0x4E, 0xF, 0xF, true);
  return v4u{q ? d2 : y0, q ? d3 : y1, q ? y2 : d0, q ? y3 : d1};
}
DEV c2 cj(c2 a) { return {a.re, -a.im}; }
DEV c2 cmulj(c2 a, c2 b) { return {fma2(a.re, b.re, -(a.im * b.im)), fma2(-a.re, b.im, -(a.im * b.re))}; }

__global__ __launch_bounds__(512, 2) void fused_ssm(
    const u16* __restrict__ x, const u16* __restrict__ Lre, const u16* __restrict__ Lim, const u16* __restrict__ Bre,
    const u16* __restrict__ Bim, const u16* __restrict__ Cre, const u16* __restrict__ Cim, const u16* __restrict__ ldtA,
    const u16* __restrict__ DA, const u16* __restrict__ u, const u16* __restrict__ lA, const u16* __restrict__ Aim,
    const u16* __restrict__ Cr, const u16* __restrict__ Ci, const u16* __restrict__ ldtB, const u16* __restrict__ DB,
    float* out, char* ws) {
#if defined(__HIP_DEVICE_COMPILE__)
  __shared__ __attribute__((aligned(16))) char smem_[163840];
  LAS char* smem = (LAS char*)smem_;
  u64 pc0;
  asm volatile("s_getpc_b64 %0" : "=s"(pc0));
  const int b = blockIdx.x, xcd = b & 7, j = b >> 3;
  const int tid = threadIdx.x, lane = tid & 63, w = __builtin_amdgcn_readfirstlane(tid >> 6);
  const int r = lane & 15, g = lane >> 4;
  STAMP(0);

  if (__builtin_expect(j >= 18, 0)) {
  if (j < 27) {
    const int d0 = 72 * xcd + 8 * (j - 18), d = d0 + w;
    LAS char* sc = smem + w * 20464;
    const int tb = ((tid >> 1) + 32 * (j - 18)) & 255, hf = tid & 1;
    const unsigned qa = *(const GAS unsigned*)(lA + d * 128 + 2 * lane), qi = *(const GAS unsigned*)(Aim + d * 128 + 2 * lane);
    const unsigned qr = *(const GAS unsigned*)(Cr + d * 128 + 2 * lane), qc = *(const GAS unsigned*)(Ci + d * 128 + 2 * lane);
    const v2u dk = *(const GAS v2u*)(DB + d0 + 4 * hf);
    const float dt = rbf(__builtin_amdgcn_exp2f(bflo(ldtB[d]) * LOG2E));
    const f32x2 l2 = bf2(pk2(exp2v(bf2(qa) * LOG2E))) * (-dt) * LOG2E, rv = redrev(bf2(qi) * dt);
    FENCE();
    STAMP(1);
    v2u uv[8];
    const auto rsU = __builtin_amdgcn_make_buffer_rsrc((void*)u, 0, 2048 * 1152, 0x00020000);
    const unsigned uvo = (8 * tb) * 1152 + (d0 + 4 * hf) * 2;
#define ULOAD(i) do { FENCE(); uv[i] = __builtin_amdgcn_raw_buffer_load_b64(rsU, uvo, (i) * 1152, 0); FENCE(); } while (0)
    ULOAD(0); ULOAD(1);
    f32x4 kacc[3] = {};
    {
      const c2 w1 = cpow(l2, rv, sp(1.f)), v8 = cpow(l2, -rv, sp(8.f)), Q = cpow(l2, rv, sp(128.f));
      LAS char* tw = sc + lane * 8;
      LAS char* tr = sc + r * 528 + g * 16;
#define PUTG(row, z) (*(LAS v2u*)(tw + (row) * 528) = v2u{pk(z.re[0], z.im[0]), pk(z.re[1], z.im[1])})
      *(LAS v2u*)(tw) = v2u{0x3F80u, 0x3F80u};
      c2 f = v8;
      PUTG(1, f);
#pragma unroll
      for (int i = 2; i < 16; ++i) { f = cmul(f, v8); PUTG(i, f); if (i == 4) ULOAD(2); if (i == 8) ULOAD(3); if (i == 12) ULOAD(4); }
      ULOAD(5);
      v4u fF[8];
#pragma unroll
      for (int ks = 0; ks < 8; ++ks) fF[ks] = lds128(tr + ks * 64);
      c2 R[8];
      R[0] = {bf2(qr) * (2.f * dt), bf2(qc) * (2.f * dt)};
#pragma unroll
      for (int i = 1; i < 8; ++i) R[i] = cmul(R[i - 1], w1);
      ULOAD(6);
#pragma unroll
      for (int i = 0; i < 8; ++i) { PUTG(i, R[i]); R[i] = cmul(R[i], Q); PUTG(8 + i, R[i]); if (i == 2) ULOAD(7); }
#pragma unroll
      for (int ks = 0; ks < 8; ++ks) kacc[0] = mfma(lds128(tr + ks * 64), fF[ks], kacc[0]);
#pragma unroll
      for (int cc = 0; cc < 4; ++cc) {
        const unsigned sel = (cc & 1) ? 0x07060302u : 0x05040100u;
        v4u o;
#pragma unroll
        for (int k = 0; k < 4; ++k) o[k] = perm(uv[2 * k + 1][cc >> 1], uv[2 * k][cc >> 1], sel);
        LAS char* ub = smem + (4 * hf + cc) * 20464 + 8448;
        *(LAS v4u*)(ub + 4640 + (8 * tb + 224) * 2) = o;
        LAS unsigned* u0 = (LAS unsigned*)(ub + (8 * tb + 226) * 2);
        u0[0] = o[0]; u0[1] = o[1]; u0[2] = o[2]; u0[3] = o[3];
      }
      STAMP(2);
      BARRIER();
      if (w < 4) __builtin_amdgcn_s_setprio(1);
#pragma unroll
      for (int s = 0; s < 2; ++s)
#pragma unroll
        for (int i = 0; i < 4; ++i) { R[2 * i] = cmul(R[2 * i], Q); PUTG(4 * s + i, R[2 * i]); }
#pragma unroll
      for (int s = 0; s < 4; ++s)
#pragma unroll
        for (int i = 0; i < 2; ++i) { R[4 * i] = cmul(R[4 * i], Q); PUTG(8 + 2 * s + i, R[4 * i]); }
#pragma unroll
      for (int ks = 0; ks < 8; ++ks) kacc[1] = mfma(lds128(tr + ks * 64), fF[ks], kacc[1]);
#pragma unroll
      for (int s = 0; s < 9; ++s) { R[0] = cmul(R[0], Q); PUTG(s, R[0]); }
#pragma unroll
      for (int ks = 0; ks < 8; ++ks) kacc[2] = mfma(lds128(tr + ks * 64), fF[ks], kacc[2]);
    }
    FENCE();
    STAMP(3);
    {
      LAS char* tt = sc + 17728;
      const unsigned aA = g < 2 ? (4 * r + 64 * g) * 2 : (128 + 2 * r + 64 * (g - 2)) * 2;
      *(LAS unsigned*)(tt + aA) = pk(kacc[1][0], kacc[1][1]);
      *(LAS unsigned*)(tt + aA + (g < 2 ? 4 : 64)) = pk(kacc[1][2], kacc[1][3]);
      const unsigned k01 = pk(kacc[2][0], kacc[2][1]), k23 = pk(kacc[2][2], kacc[2][3]);
      LAS char* t3 = tt + (256 + 64 * g + r) * 2;
      *(LAS u16*)(t3) = (u16)k01; *(LAS u16*)(t3 + 32) = (u16)(k01 >> 16);
      *(LAS u16*)(t3 + 64) = (u16)k23; *(LAS u16*)(t3 + 96) = (u16)(k23 >> 16);
      if (lane < 17) *(LAS unsigned*)(sc + (1023 + lane) * 4) = 0u;
      if (lane < 16) *(LAS unsigned*)(sc + 4224 + (1024 + lane) * 4) = 0u;
      if (lane < 28) { *(LAS v4u*)(sc + 8448 + lane * 16) = v4u{0, 0, 0, 0}; *(LAS v4u*)(sc + 8448 + 4640 + lane * 16) = v4u{0, 0, 0, 0}; }
      if (lane == 28) *(LAS unsigned*)(sc + 8448 + 448) = 0u;
      FENCE();
      const unsigned a1 = *(LAS unsigned*)(tt + lane * 4), b1 = *(LAS unsigned*)(tt + lane * 4 + 4);
      const unsigned a2 = *(LAS unsigned*)(tt + 256 + lane * 4), b2 = *(LAS unsigned*)(tt + 256 + lane * 4 + 4);
      const unsigned a3 = *(LAS unsigned*)(tt + 512 + lane * 4), b3 = *(LAS unsigned*)(tt + 512 + lane * 4 + 4);
      {
        const unsigned e0 = perm(a1, a1, 0x01000100u), m10 = perm(a1, a1, 0x01000302u), m21 = perm(a1, b1, 0x07060100u);
        *(LAS v2u*)(sc + 4224 + (1788 - 4 * lane) * 2) = v2u{m21, e0};
        *(LAS v2u*)(sc + (1788 - 4 * lane) * 2) = v2u{m10, e0};
      }
      {
        const unsigned e0 = perm(a2, a2, 0x01000100u), e1 = perm(a2, a2, 0x03020302u), e2 = perm(b2, b2, 0x01000100u);
        const unsigned m10 = perm(a2, a2, 0x01000302u), m21 = perm(a2, b2, 0x07060100u);
        *(LAS v4u*)(sc + 4224 + (1528 - 8 * lane) * 2) = v4u{e2, e1, m10, e0};
        *(LAS v4u*)(sc + (1528 - 8 * lane) * 2) = v4u{m21, e1, e0, e0};
      }
      {
        const unsigned e0 = perm(a3, a3, 0x01000100u), e1 = perm(a3, a3, 0x03020302u), e2 = perm(b3, b3, 0x01000100u);
        const unsigned m10 = perm(a3, a3, 0x01000302u), m21 = perm(a3, b3, 0x07060100u);
        LAS char* p1 = sc + 4224 + (1008 - 16 * lane) * 2;
        LAS char* p0_ = sc + (1008 - 16 * lane) * 2;
        *(LAS v4u*)(p1) = v4u{e2, e2, e1, e1}; *(LAS v4u*)(p1 + 16) = v4u{e1, m10, e0, e0};
        *(LAS v4u*)(p0_) = v4u{e2, m21, e1, e1}; *(LAS v4u*)(p0_ + 16) = v4u{e1, e0, e0, e0};
      }
      FENCE();
      {
        const int q0 = 8 * r + 4 * (g & 1) + 128 * (g >> 1);
        const unsigned v32 = pk(kacc[0][3], kacc[0][2]), v10 = pk(kacc[0][1], kacc[0][0]), v21 = pk(kacc[0][2], kacc[0][1]);
        *(LAS v2u*)(sc + 4224 + (2044 - q0) * 2) = v2u{v32, v10};
        *(LAS u16*)(sc + (2043 - q0) * 2) = (u16)v32;
        *(LAS unsigned*)(sc + (2044 - q0) * 2) = v21;
        *(LAS u16*)(sc + (2046 - q0) * 2) = (u16)(v10 >> 16);
      }
    }
    FENCE();
    STAMP(4);
    {
      f32x4 ya[8] = {};
      LAS char* pT = sc + (r & 1) * 4224 + 504 - 8 * (r >> 1) + 16 * g;
      LAS char* pT2 = pT + 8;
      asm volatile("" : "+v"(pT2));
      LAS char* pU = sc + 8448 + (r & 1) * 4640 + 448 + 64 * (r >> 1) + 16 * g;
      v4u U[8], Tq[2];
#define LDT(off) ({ const v2u a_ = lds64(pT + (off)), b_ = lds64(pT2 + (off)); v4u{a_[0], a_[1], b_[0], b_[1]}; })
      Tq[0] = LDT(512 * 7);
#pragma unroll
      for (int k = 7; k >= 0; --k) U[k] = lds128(pU + 512 * k);
      Tq[1] = LDT(512 * 6);
#pragma unroll 1
      for (int rr = 0; rr < 8; ++rr) {
#pragma unroll
        for (int m = 0; m < 8; ++m) {
          const v4u T = Tq[m & 1];
          Tq[m & 1] = m < 6 ? LDT(512 * (5 - m)) : LDT(512 * (13 - m) - 64);
#pragma unroll
          for (int k = 7 - m; k >= 0; --k) ya[k + m] = mfma(T, U[k], ya[k + m]);
          U[7 - m] = lds128(pU + 512 * (7 - m) - 64);
          SCHED();
        }
        pT -= 64; pT2 -= 64; pU -= 64;
      }
      FENCE();
      LAS char* py = sc + (4 * g + (r & 1) + 16 * (r >> 1)) * 4;
#pragma unroll
      for (int q = 0; q < 8; ++q) {
        *(LAS unsigned*)(py + 512 * q) = pk(ya[q][0], ya[q][1]);
        *(LAS unsigned*)(py + 512 * q + 8) = pk(ya[q][2], ya[q][3]);
      }
    }
    STAMP(5);
    BARRIER();
    {
      v4u yv[4];
#pragma unroll
      for (int cc = 0; cc < 4; ++cc) yv[cc] = lds128(smem + (4 * hf + cc) * 20464 + 16 * tb);
      const auto rsO = __builtin_amdgcn_make_buffer_rsrc(out, 0, 2048 * 1152 * 4, 0x00020000);
      const unsigned op = ((8 * tb) * 1152 + 576 + d0 + 4 * hf) * 4;
      const f32x2 d01 = bf2(dk[0]), d23 = bf2(dk[1]);
#pragma unroll
      for (int i = 0; i < 8; ++i) {
        const f32x2 s01 = bf2(pk2(bf2(uv[i][0]) * d01)), s23 = bf2(pk2(bf2(uv[i][1]) * d23));
        f32x2 y01, y23;
        if (i & 1) { y01 = f32x2{bfhi(yv[0][i >> 1]), bfhi(yv[1][i >> 1])}; y23 = f32x2{bfhi(yv[2][i >> 1]), bfhi(yv[3][i >> 1])}; }
        else { y01 = f32x2{bflo(yv[0][i >> 1]), bflo(yv[1][i >> 1])}; y23 = f32x2{bflo(yv[2][i >> 1]), bflo(yv[3][i >> 1])}; }
        const f32x2 o01 = bf2(pk2(y01 + s01)), o23 = bf2(pk2(y23 + s23));
        __builtin_amdgcn_raw_buffer_store_b128(__builtin_bit_cast(v4u, f32x4{o01[0], o01[1], o23[0], o23[1]}), rsO, op + i * 4608, 0, AUXB);
      }
    }
    STAMP(15);
    STAMP(14);
    STAMP_FLUSH();
  } else if (b != 255) {
    unsigned v = 0;
    if (j == 27 && tid < CODE_LINES) v = *(const GAS unsigned*)((pc0 & ~127ull) + tid * 128);
    if (j < 31) {
      const int R = ((j - 27) * 8 + w) * 36 + (lane < 36 ? lane : 35);
      const u64 bx = (u64)x + (xcd >> 1) * 512 * 1152, br = (u64)Bre + ((xcd & 1) * 256 - 512) * 1152, bi = (u64)Bim + ((xcd & 1) * 256 - 832) * 1152;
      const u64 base = (R < 512 ? bx : (R < 832 ? br : bi)) + R * 1152;
      unsigned t[9];
#pragma unroll
      for (int k = 0; k < 9; ++k) t[k] = *(const GAS unsigned*)(base + k * 128);
#pragma unroll
      for (int k = 0; k < 9; ++k) v ^= t[k];
    }
    asm volatile("" ::"v"(v));
  } else if (w == 0) {
    const u64 tag = *(const CAS u64*)ws + TAG_STEP;
    const GAS u64* fl = (const GAS u64*)(ws + WS_FLAG);
#pragma unroll 1
    for (unsigned sp_ = 0; sp_ < SPIN_CAP; ++sp_) {
      __builtin_amdgcn_s_sleep(16);
      FENCE();
      const u64 f0 = __hip_atomic_load(fl + lane * 16, __ATOMIC_RELAXED, __HIP_MEMORY_SCOPE_AGENT);
      const u64 f1 = __hip_atomic_load(fl + (64 + lane) * 16, __ATOMIC_RELAXED, __HIP_MEMORY_SCOPE_AGENT);
      const u64 f2 = __hip_atomic_load(fl + (128 + (lane & 15)) * 16, __ATOMIC_RELAXED, __HIP_MEMORY_SCOPE_AGENT);
      if (__all(f0 == tag && f1 == tag && f2 == tag)) break;
    }
    if (lane == 0) __hip_atomic_store((GAS u64*)ws, tag, __ATOMIC_RELAXED, __HIP_MEMORY_SCOPE_AGENT);
  }
  return;
  }
  {
    const int odd = xcd & 1, mid = j >> 4;
    const int pi = mid ? (j - 16) + 2 * odd : (j >> 2), gi = mid ? 4 : (j & 3);
    const int pg = mid ? 4 : gi + 5 * odd, c = 4 * (xcd >> 1) + pi;
    const int wm = w & 3, wn = w >> 2;
    const int p0 = 64 * pg + 32 * wn + 2 * r;
    const unsigned pLr = *(const GAS unsigned*)(Lre + p0), pLi = *(const GAS unsigned*)(Lim + p0), pDt = *(const GAS unsigned*)(ldtA + p0);
    FENCE();
    const u64 sx = (u64)x + (c * 128 + w * 32) * 1152, sb = (u64)(w < 6 ? Bre : Bim) + (pg * 64 + (w & 1) * 32) * 1152;
    const auto rsS = __builtin_amdgcn_make_buffer_rsrc((void*)(wn ? sb : sx), 0, 32 * 1152, 0x00020000);
    const int l3 = lane >> 3, ch = (lane & 7) ^ (l3 >> 1);
    const int rowx = 8 * (l3 >> 2) + 4 * (l3 & 1) + ((l3 >> 1) & 1), rowb = rowx ^ ((rowx ^ (2 * l3)) & -wn);
    const unsigned dvA = __umul24(rowb, 1152u) + ch * 16, dvB = dvA + 16 * 1152 + 64 - 2 * (dvA & 64);
    const int stp = (2 - wn) * 1152;
#define SLOT(s) (((8 - (s)) % 3) * 32768)
#define SPIECE(s, i) __builtin_amdgcn_raw_ptr_buffer_load_lds(rsS, smem + SLOT(s) + w * 4096 + (i) * 1024, 16, ((i) & 1) ? dvB : dvA, (s) * 128 + ((i) >> 1) * stp, 0, 0)
    SPIECE(0, 0); SPIECE(0, 1); SPIECE(0, 2); SPIECE(0, 3);
    asm volatile("s_barrier" ::: "memory");
    SPIECE(1, 0); SPIECE(1, 1); SPIECE(1, 2); SPIECE(1, 3);
    FENCE();
    const u64 tag = *(const CAS u64*)ws + TAG_STEP;
    const unsigned tagA = (unsigned)tag, tagB = (unsigned)(tag >> 32);
    const auto rsW = __builtin_amdgcn_make_buffer_rsrc(ws, 0, WS_END, 0x00020000);
    const int c2_ = 2 * xcd + (j >= 9), n = j >= 9 ? j - 9 : j;
    const auto rsCre = __builtin_amdgcn_make_buffer_rsrc((void*)(Cre + 64 * n * 576), 0, 64 * 1152, 0x00020000);
    const auto rsCim = __builtin_amdgcn_make_buffer_rsrc((void*)(Cim + 64 * n * 576), 0, 64 * 1152, 0x00020000);
    const unsigned cvo = __umul24(32 * (w & 1) + 4 * l3 + (w >> 1), 1152u) + (((lane & 7) ^ ((4 * w + (l3 >> 1)) & 7)) * 16);
#define CPIECE(q) __builtin_amdgcn_raw_ptr_buffer_load_lds((q) < 9 ? rsCre : rsCim, smem + 16384 + (q) * 8192 + w * 1024, 16, cvo, ((q) % 9) * 128, 0, 0)

    const f32x2 lr = bf2(pLr), li = bf2(pLi), dtA = bf2(pk2(exp2v(bf2(pDt) * LOG2E)));
    c2 lam = cpow(dtA * lr * LOG2E, redrev(dtA * li), sp(1.f));
    c2 l4 = csqr(csqr(lam)), l8 = csqr(l4), l16 = csqr(l8), l32 = csqr(l16);
    asm volatile("" : "+v"(lam.re), "+v"(lam.im), "+v"(l4.re), "+v"(l4.im), "+v"(l8.re), "+v"(l8.im), "+v"(l16.re), "+v"(l16.im), "+v"(l32.re), "+v"(l32.im));

    const unsigned fo = r * 128 + ((g ^ (r >> 1)) * 16);
    LAS char* pa0 = smem + wm * 4096 + fo;
    LAS char* pa1 = smem + wm * 4096 + (fo ^ 64);
    LAS char* pb0 = smem + 16384 + wn * 4096 + fo;
    LAS char* pb1 = smem + 16384 + wn * 4096 + (fo ^ 64);
    f32x4 acc[2][2][2] = {};
    v4u fr[2][12];
    STAMP(1);
#pragma unroll
    for (int s = 0; s < 10; ++s) {
      if (s <= 6) WAITV(4); else if (s <= 8) WAITV(8);
      if (s == 1) asm volatile("" ::"s"(tagA), "s"(tagB));
      BARRIER();
      if (s == 0) STAMP(2);
      if (s == 3) STAMP(11);
      if (s == 6) STAMP(12);
      if (s == 9) STAMP(13);
#pragma unroll
      for (int i = 0; i < 16; ++i) {
        if (s < 9 && i < 12) {
          const int kk = i / 6, e = i % 6;
          LAS char* pa = (kk ? pa1 : pa0) + SLOT(s);
          LAS char* pb = (kk ? pb1 : pb0) + SLOT(s);
          fr[s & 1][i] = e == 0 ? lds128(pa) : e == 5 ? lds128(pa + 2048) : lds128(pb + ((e - 1) >> 1) * 8192 + ((e - 1) & 1) * 2048);
        }
        if (s >= 1) {
          const int kk = i >> 3, m = (i >> 2) & 1, e = i & 3;
          acc[m][e >> 1][e & 1] = mfma(fr[(s - 1) & 1][6 * kk + 5 * m], fr[(s - 1) & 1][6 * kk + 1 + e], acc[m][e >> 1][e & 1]);
        }
        if ((i & 3) == 3) {
          if (s + 2 < 9) SPIECE(s + 2, i >> 2);
          if (s == 7) CPIECE(14 + (i >> 2));
          if (s == 8) CPIECE(6 + (i >> 2));
        }
        SCHED();
      }
      if (s == 6) { CPIECE(10); CPIECE(11); CPIECE(12); CPIECE(13); SCHED(); }
    }
    STAMP(3);
#define BSTEP(t, s) c2{f32x2{acc[(s) >> 1][0][t][2 * ((s) & 1)], acc[(s) >> 1][0][t][2 * ((s) & 1) + 1]}, f32x2{acc[(s) >> 1][1][t][2 * ((s) & 1)], acc[(s) >> 1][1][t][2 * ((s) & 1) + 1]}}
    c2 eA, ea, ea2;
    LAS char* sW = smem;
    LAS char* sH = smem + 2048;
    {
      c2 e2[2];
#pragma unroll
      for (int t = 0; t < 2; ++t) {
        e2[t] = BSTEP(t, 0);
#pragma unroll
        for (int s = 1; s < 4; ++s) e2[t] = cfma(el(lam, t), e2[t], BSTEP(t, s));
      }
      eA = {f32x2{e2[0].re[0], e2[1].re[0]}, f32x2{e2[0].im[0], e2[1].im[0]}};
      const c2 e = cfma(l4, eA, c2{f32x2{e2[0].re[1], e2[1].re[1]}, f32x2{e2[0].im[1], e2[1].im[1]}});
      c2 hi;
      ea.re = swap16(e.re, hi.re); ea.im = swap16(e.im, hi.im);
      const c2 A = cfma(l8, ea, hi);
      ea2.re = swap32(A.re, hi.re); ea2.im = swap32(A.im, hi.im);
      const c2 W = cfma(l16, ea2, hi);
      if (g == 0) *(LAS f32x4*)(sW + (wm * 32 + 16 * wn + r) * 16) = f32x4{W.re[0], W.re[1], W.im[0], W.im[1]};
    }
    const bool pub = w == 0 || w == 5;
    if (pub) __builtin_amdgcn_s_setprio(2);
    BARRIER();
    c2 pw;
    {
      f32x4 wv[4];
#pragma unroll
      for (int i = 0; i < 4; ++i) wv[i] = *(LAS f32x4*)(sW + (i * 32 + 16 * wn + r) * 16);
      const c2 s1 = {wv[0].xy, wv[0].zw};
      const c2 s2 = cfma(l32, s1, c2{wv[1].xy, wv[1].zw});
      const c2 s3 = cfma(l32, s2, c2{wv[2].xy, wv[2].zw});
      if (pub) {
        const c2 E = cfma(l32, s3, c2{wv[3].xy, wv[3].zw});
        if (g < 2)
          __builtin_amdgcn_raw_buffer_store_b128(v4u{__float_as_uint(g ? E.re[1] : E.re[0]), tagA, __float_as_uint(g ? E.im[1] : E.im[0]), tagB}, rsW,
                                                 WS_E + (c * 576 + p0 + g) * 16, 0, 16);
        __builtin_amdgcn_s_setprio(0);
      }
      pw.re = wm == 0 ? sp(0.f) : wm == 1 ? s1.re : wm == 2 ? s2.re : s3.re;
      pw.im = wm == 0 ? sp(0.f) : wm == 1 ? s1.im : wm == 2 ? s2.im : s3.im;
    }
    FENCE();
    STAMP(4);
    const unsigned qLr = Lre[64 * pg + lane], qLi = Lim[64 * pg + lane], qDt = ldtA[64 * pg + lane];
    FENCE();
    CPIECE(0); CPIECE(1); CPIECE(2); CPIECE(3); CPIECE(4); CPIECE(5);
    FENCE();
    const int t2 = 128 * c2_ + 32 * (w >> 1) + 4 * (w & 1) + 8 * g;
    v2u xs[4];
#pragma unroll
    for (int i = 0; i < 4; ++i) xs[i] = *(const GAS v2u*)(x + (t2 + i) * 576 + 64 * n + 4 * r);
    const v2u dsk = *(const GAS v2u*)(DA + 64 * n + 4 * r);
    FENCE();
    c2 pf2[2], lp2[2], bs[2][4];
    {
      const f32x2 den = lr * lr + li * li, inv = f32x2{__builtin_amdgcn_rcpf(den[0]), __builtin_amdgcn_rcpf(den[1])} * 2.f, nr = lam.re - 1.f;
      const c2 coef = {(nr * lr + lam.im * li) * inv, (lam.im * lr - nr * li) * inv};
      const c2 z = {sp(0.f), sp(0.f)}, one = {sp(1.f), sp(0.f)};
      const c2 lg = cmul((g & 1) ? l8 : one, (g & 2) ? l16 : one);
      const c2 lwv = cmul((wm & 1) ? l32 : one, (wm & 2) ? csqr(l32) : one);
      const c2 lw = cmul(cmul(coef, lg), lwv);
      const c2 o = (g & 2) ? ea2 : z;
      const c2 pin = (g & 1) ? cfma(l8, o, ea) : o;
      const c2 pfA = cmul(coef, cfma(lg, pw, pin));
      const c2 pfB = cfma(l4, pfA, cmul(coef, eA)), lwB = cmul(l4, lw);
#pragma unroll
      for (int t = 0; t < 2; ++t) {
        pf2[t] = {f32x2{pfA.re[t], pfB.re[t]}, f32x2{-pfA.im[t], -pfB.im[t]}};
        lp2[t] = {f32x2{lw.re[t], lwB.re[t]}, f32x2{-lw.im[t], -lwB.im[t]}};
#pragma unroll
        for (int s = 0; s < 4; ++s) {
          bs[t][s] = cmulj(el(coef, t), BSTEP(t, s));
          asm volatile("" : "+v"(bs[t][s].re), "+v"(bs[t][s].im));
        }
        asm volatile("" : "+v"(pf2[t].re), "+v"(pf2[t].im), "+v"(lp2[t].re), "+v"(lp2[t].im));
      }
    }
    if (w < 4) {
      float hr = 0.f, hi = 0.f;
      if (w < c) {
        const float dt = rbf(__builtin_amdgcn_exp2f(bflo(qDt) * LOG2E));
        const f32x2 ql2 = sp(dt * bflo(qLr) * LOG2E), qrv = redrev(sp(dt * bflo(qLi)));
        const int n0 = c - 1 - w;
        const c2 wa = cpow(ql2, qrv, f32x2{(float)(128 * n0), (float)(128 * (n0 > 4 ? n0 - 4 : 0))});
        const c2 wb = cpow(ql2, qrv, f32x2{(float)(128 * (n0 > 8 ? n0 - 8 : 0)), (float)(128 * (n0 > 12 ? n0 - 12 : 0))});
        const unsigned eo = WS_E + (w * 576 + 64 * pg + lane) * 16;
        const unsigned o1 = n0 >= 4 ? 4 * 9216 : 0, o2 = n0 >= 8 ? 8 * 9216 : 0, o3 = n0 >= 12 ? 12 * 9216 : 0;
        const f32x2 ma = {1.f, n0 >= 4 ? 1.f : 0.f}, mb = {n0 >= 8 ? 1.f : 0.f, n0 >= 12 ? 1.f : 0.f};
        c2 wa2 = {wa.re * ma, wa.im * ma}, wb2 = {wb.re * mb, wb.im * mb};
        asm volatile("" : "+v"(wa2.re), "+v"(wa2.im), "+v"(wb2.re), "+v"(wb2.im));
        v4u e0, e1, e2, e3;
#pragma unroll 1
        for (unsigned sp_ = 0; sp_ < SPIN_CAP; ++sp_) {
          FENCE();
          e0 = __builtin_amdgcn_raw_buffer_load_b128(rsW, eo, 0, 16);
          e1 = __builtin_amdgcn_raw_buffer_load_b128(rsW, eo, o1, 16);
          e2 = __builtin_amdgcn_raw_buffer_load_b128(rsW, eo, o2, 16);
          e3 = __builtin_amdgcn_raw_buffer_load_b128(rsW, eo, o3, 16);
          const unsigned bad = ((e0[1] ^ tagA) | (e0[3] ^ tagB) | (e1[1] ^ tagA)) | ((e1[3] ^ tagB) | (e2[1] ^ tagA) | (e2[3] ^ tagB)) | ((e3[1] ^ tagA) | (e3[3] ^ tagB));
          if (__all(bad == 0)) break;
          __builtin_amdgcn_s_sleep(1);
        }
        const c2 hp = cfma(wa2, c2{f32x2{__uint_as_float(e0[0]), __uint_as_float(e1[0])}, f32x2{__uint_as_float(e0[2]), __uint_as_float(e1[2])}},
                           cmul(wb2, c2{f32x2{__uint_as_float(e2[0]), __uint_as_float(e3[0])}, f32x2{__uint_as_float(e2[2]), __uint_as_float(e3[2])}}));
        hr = hp.re[0] + hp.re[1]; hi = hp.im[0] + hp.im[1];
      }
      LAS float* hq = (LAS float*)(sH + (w * 32 + (lane >> 1)) * 16) + (lane & 1);
      hq[0] = hr; hq[2] = -hi;
    }
    STAMP(5);
    BARRIER();
    {
      const f32x4 hs = (*(LAS f32x4*)(sH + (16 * wn + r) * 16) + *(LAS f32x4*)(sH + (32 + 16 * wn + r) * 16)) +
                       (*(LAS f32x4*)(sH + (64 + 16 * wn + r) * 16) + *(LAS f32x4*)(sH + (96 + 16 * wn + r) * 16));
      const c2 H = {hs.xy, hs.zw}, lamj = cj(lam);
      c2 h0 = cfma(lp2[0], el(H, 0), pf2[0]), h1 = cfma(lp2[1], el(H, 1), pf2[1]);
      unsigned dv[4][4];
#pragma unroll
      for (int s = 0; s < 4; ++s) {
        h0 = cfma(el(lamj, 0), h0, bs[0][s]);
        h1 = cfma(el(lamj, 1), h1, bs[1][s]);
        dv[0][s] = pk(h0.re[0], h1.re[0]); dv[2][s] = pk(h0.re[1], h1.re[1]);
        dv[1][s] = pk(h0.im[0], h1.im[0]); dv[3][s] = pk(h0.im[1], h1.im[1]);
      }
      LAS char* tb_ = smem + 8192 + w * 1024;
      LAS char* tw_ = tb_ + ((r >> 2) * 16 + 4 * g) * 16 + (r & 3) * 4;
      LAS char* tr_ = tb_ + (lane ^ g) * 16;
      const unsigned hoff = WS_H + (4 * c + wm) * 2 * 36864 + (2 * pg + wn) * 2048 + lane * 16;
#pragma unroll
      for (int e = 0; e < 4; ++e) {
#pragma unroll
        for (int s = 0; s < 4; ++s) *(LAS unsigned*)(tw_ + ((s ^ (r >> 2)) * 16)) = dv[e][s];
        FENCE();
        const v4u o = lds128(tr_);
        FENCE();
        __builtin_amdgcn_raw_buffer_store_b128(o, rsW, hoff + (e >> 1) * 36864 + (e & 1) * 1024, 0, 16);
      }
    }
    STAMP(6);
    WAITV(0);
    BARRIER();
    STAMP(7);
    if (w == 0) {
      if (lane == 0) __hip_atomic_store((GAS u64*)(ws + WS_FLAG) + (c * 9 + pg) * 16, tag, __ATOMIC_RELAXED, __HIP_MEMORY_SCOPE_AGENT);
      const GAS u64* fp = (const GAS u64*)(ws + WS_FLAG) + (c2_ * 9 + (lane < 9 ? lane : 0)) * 16;
#pragma unroll 1
      for (unsigned sp_ = 0; sp_ < SPIN_CAP; ++sp_) {
        FENCE();
        if (__all(__hip_atomic_load(fp, __ATOMIC_RELAXED, __HIP_MEMORY_SCOPE_AGENT) == tag)) break;
        __builtin_amdgcn_s_sleep(1);
      }
    }
    BARRIER();
    STAMP(8);
    {
      constexpr int HD = 8;
      const unsigned hsrc = WS_H + (c2_ * 8 + w) * 36864;
      v4u hf[HD];
#pragma unroll
      for (int i = 0; i < HD; ++i) hf[i] = __builtin_amdgcn_raw_buffer_load_b128(rsW, lane * 16, hsrc + i * 1024, 16);
      LAS char* pc0_ = smem + 16384 + fo;
      LAS char* pc1_ = smem + 16384 + (fo ^ 64);
      f32x4 a[4] = {};
      v4u cf[2][4];
#define CFRAG(k1, jt) lds128(((((k1) >> 1) & 1) ? pc1_ : pc0_) + (((k1) & 1) * 9 + ((k1) >> 2)) * 8192 + (jt) * 2048)
#pragma unroll
      for (int jt = 0; jt < 4; ++jt) cf[0][jt] = CFRAG(0, jt);
#pragma unroll
      for (int ks = 0; ks < 36; ++ks) {
        const v4u hc = hf[ks % HD];
#pragma unroll
        for (int jt = 0; jt < 4; ++jt) {
          if (ks + 1 < 36) cf[(ks + 1) & 1][jt] = CFRAG(ks + 1, jt);
          a[jt] = mfma(hc, cf[ks & 1][jt], a[jt]);
          SCHED();
        }
        if (ks + HD < 36) hf[ks % HD] = __builtin_amdgcn_raw_buffer_load_b128(rsW, lane * 16, hsrc + (ks + HD) * 1024, 16);
        SCHED();
        if (ks == 0) STAMP(9);
      }
      STAMP(10);
      const auto rsO = __builtin_amdgcn_make_buffer_rsrc(out, 0, 2048 * 1152 * 4, 0x00020000);
      const f32x2 d01 = bf2(dsk[0]), d23 = bf2(dsk[1]);
#pragma unroll
      for (int i = 0; i < 4; ++i) {
        const f32x2 s01 = bf2(pk2(bf2(xs[i][0]) * d01)), s23 = bf2(pk2(bf2(xs[i][1]) * d23));
        __builtin_amdgcn_raw_buffer_store_b128(__builtin_bit_cast(v4u, f32x4{a[0][i] + s01[0], a[1][i] + s01[1], a[2][i] + s23[0], a[3][i] + s23[1]}), rsO,
                                               ((t2 + i) * 1152 + 64 * n + 4 * r) * 4, 0, AUXA);
      }
    }
    STAMP(15);
    STAMP(14);
    STAMP_FLUSH();
  }
#endif
}

__global__ void dummy_k(char* ws) { if (ws == nullptr) return; }
extern "C" void kernel_launch(void* const* d_in, const int* in_sizes, int n_in, void* d_out, int out_size, void* d_ws,
                              size_t ws_size, hipStream_t stream) {
  fused_ssm<<<256, 512, 0, stream>>>((const u16*)d_in[0], (const u16*)d_in[1], (const u16*)d_in[2], (const u16*)d_in[3],
                                     (const u16*)d_in[4], (const u16*)d_in[5], (const u16*)d_in[6], (const u16*)d_in[7],
                                     (const u16*)d_in[8], (const u16*)d_in[9], (const u16*)d_in[10], (const u16*)d_in[11],
                                     (const u16*)d_in[12], (const u16*)d_in[13], (const u16*)d_in[14], (const u16*)d_in[15],
                                     (float*)d_out, (char*)d_ws);
  dummy_k<<<1, 64, 0, stream>>>((char*)d_ws);
}
```

```cpp
#include <hip/hip_runtime.h>
#include <stdint.h>

typedef unsigned short u16;
typedef unsigned long long u64;
typedef unsigned v4u __attribute__((ext_vector_type(4)));
typedef unsigned v2u __attribute__((ext_vector_type(2)));
typedef float f32x4 __attribute__((ext_vector_type(4)));
typedef float f32x2 __attribute__((ext_vector_type(2)));
typedef __bf16 bf16x8 __attribute__((ext_vector_type(8)));
typedef __bf16 bf16x2 __attribute__((ext_vector_type(2)));
#define CAS __attribute__((address_space(4)))
#define LAS __attribute__((address_space(3)))
#define GAS __attribute__((address_space(1)))
#define DEV __device__ __forceinline__

constexpr int WS_FLAG = 4096;
constexpr int WS_ZFLAG = WS_FLAG + 144 * 128;
constexpr int WS_TFLAG = WS_ZFLAG + 72 * 128;
constexpr int WS_YFLAG = WS_TFLAG + 36 * 128;
constexpr int WS_E = 98304;
constexpr int WS_DONE = WS_E + 16 * 576 * 16;
constexpr int WS_H = 524288;
constexpr int WS_UT = WS_H + 2048 * 1152 * 2;
constexpr int UTS = 4224;
constexpr int WS_YT = WS_UT + 576 * UTS;
constexpr int WS_END = WS_YT + 576 * UTS;
constexpr int WS_DBG = 16 << 20;
constexpr unsigned SPIN_CAP = 400000u;
constexpr u64 TAG_STEP = 0x9E3779B97F4A7C15ull;
#define CODE_LINES 215
#define SBAR 1
#define TAUX 2
#define YDELAY 64
#define POLLM 0
#define DELAY1 0
#define GSLEEP 2
#define TNT 0
#define AUXA 16
#define AUXB 0
#define STAMP_ON 0
#define STAMP(i) do {} while (0)
#define STAMP_FLUSH() do {} while (0)
#define STAMP_END() do {} while (0)

#define WAITV(n) asm volatile("s_waitcnt vmcnt(%0)" ::"n"(n) : "memory")
#define BARRIER() asm volatile("s_waitcnt lgkmcnt(0)\n\ts_barrier" ::: "memory")
#define FENCE() asm volatile("" ::: "memory")
#define SCHED() __builtin_amdgcn_sched_barrier(0)
#define PIN2(z) asm volatile("" : "+v"(z.re), "+v"(z.im))
#define ALOAD(p) __hip_atomic_load(p, __ATOMIC_RELAXED, __HIP_MEMORY_SCOPE_AGENT)
#define ASTORE(p, v) __hip_atomic_store(p, v, __ATOMIC_RELAXED, __HIP_MEMORY_SCOPE_AGENT)
#define RSRC(p, bytes) __builtin_amdgcn_make_buffer_rsrc((void*)(p), 0, bytes, 0x00020000)

struct c2 { f32x2 re, im; };
DEV f32x2 fma2(f32x2 a, f32x2 b, f32x2 c) { return __builtin_elementwise_fma(a, b, c); }
DEV f32x2 sp(float a) { return f32x2{a, a}; }
DEV c2 cmul(c2 a, c2 b) { return {fma2(a.re, b.re, -(a.im * b.im)), fma2(a.re, b.im, a.im * b.re)}; }
DEV c2 cmulj(c2 a, c2 b) { return {fma2(a.re, b.re, -(a.im * b.im)), fma2(-a.re, b.im, -(a.im * b.re))}; }
DEV c2 cfma(c2 a, c2 b, c2 c) { return {fma2(a.re, b.re, fma2(-a.im, b.im, c.re)), fma2(a.re, b.im, fma2(a.im, b.re, c.im))}; }
DEV c2 csqr(c2 a) { return {fma2(a.re, a.re, -(a.im * a.im)), (a.re + a.re) * a.im}; }
DEV c2 cj(c2 a) { return {a.re, -a.im}; }
DEV c2 el(c2 a, int t) { return {sp(a.re[t]), sp(a.im[t])}; }
DEV float bflo(unsigned v) { return __uint_as_float(v << 16); }
DEV float bfhi(unsigned v) { return __uint_as_float(v & 0xFFFF0000u); }
DEV f32x2 bf2(unsigned v) { return f32x2{bflo(v), bfhi(v)}; }
DEV unsigned pk(float lo, float hi) { f32x2 v = {lo, hi}; return __builtin_bit_cast(unsigned, __builtin_convertvector(v, bf16x2)); }
DEV unsigned pk2(f32x2 v) { return __builtin_bit_cast(unsigned, __builtin_convertvector(v, bf16x2)); }
DEV float rbf(float f) { return bflo(pk(f, f)); }
constexpr float LOG2E = 1.44269504088896341f, INV2PI = 0.159154943091895336f;
DEV f32x2 exp2v(f32x2 x) { return f32x2{__builtin_amdgcn_exp2f(x[0]), __builtin_amdgcn_exp2f(x[1])}; }
DEV c2 cpow(f32x2 l2, f32x2 rev, f32x2 n) {
  const f32x2 m = exp2v(l2 * n), t0 = rev * n;
  const float ta = __builtin_amdgcn_fractf(t0[0]), tb = __builtin_amdgcn_fractf(t0[1]);
  return {m * f32x2{__builtin_amdgcn_cosf(ta), __builtin_amdgcn_cosf(tb)}, m * f32x2{__builtin_amdgcn_sinf(ta), __builtin_amdgcn_sinf(tb)}};
}
DEV f32x2 redrev(f32x2 ai) { const f32x2 r = ai * INV2PI; return r - f32x2{__builtin_rintf(r[0]), __builtin_rintf(r[1])}; }
DEV f32x4 mfma(v4u a, v4u b, f32x4 c) {
  return __builtin_amdgcn_mfma_f32_16x16x32_bf16(__builtin_bit_cast(bf16x8, a), __builtin_bit_cast(bf16x8, b), c, 0, 0, 0);
}
DEV v4u lds128(LAS char* p) { return *(LAS v4u*)p; }
DEV v2u lds64(LAS char* p) { return *(LAS v2u*)p; }
DEV unsigned perm(unsigned hi, unsigned lo, unsigned sel) { return __builtin_amdgcn_perm(hi, lo, sel); }
DEV f32x2 swap16(f32x2 v, f32x2& hi) {
  auto a = __builtin_amdgcn_permlane16_swap(__float_as_uint(v[0]), __float_as_uint(v[0]), false, false);
  auto b = __builtin_amdgcn_permlane16_swap(__float_as_uint(v[1]), __float_as_uint(v[1]), false, false);
  hi = f32x2{__uint_as_float(a[1]), __uint_as_float(b[1])};
  return f32x2{__uint_as_float(a[0]), __uint_as_float(b[0])};
}
DEV f32x2 swap32(f32x2 v, f32x2& hi) {
  auto a = __builtin_amdgcn_permlane32_swap(__float_as_uint(v[0]), __float_as_uint(v[0]), false, false);
  auto b = __builtin_amdgcn_permlane32_swap(__float_as_uint(v[1]), __float_as_uint(v[1]), false, false);
  hi = f32x2{__uint_as_float(a[1]), __uint_as_float(b[1])};
  return f32x2{__uint_as_float(a[0]), __uint_as_float(b[0])};
}
DEV v4u band(int c, int g, int lo, int n) {
  const int bits = (int)((((1u << n) - 1u) << (c + lo)) >> (8 * g));
  v4u m;
#pragma unroll
  for (int i = 0; i < 4; ++i)
    m[i] = ((unsigned)__builtin_amdgcn_sbfe(bits, 2 * i, 1) & 0xFFFFu) | ((unsigned)__builtin_amdgcn_sbfe(bits, 2 * i + 1, 1) & 0xFFFF0000u);
  return m;
}
DEV float rsum4(float a, float b, float c, float d) {
  const auto s0 = __builtin_amdgcn_permlane16_swap(__float_as_uint(a), __float_as_uint(b), false, false);
  const auto s1 = __builtin_amdgcn_permlane16_swap(__float_as_uint(c), __float_as_uint(d), false, false);
  const float p = __uint_as_float(s0[0]) + __uint_as_float(s0[1]), q = __uint_as_float(s1[0]) + __uint_as_float(s1[1]);
  const auto s2 = __builtin_amdgcn_permlane32_swap(__float_as_uint(p), __float_as_uint(q), false, false);
  return __uint_as_float(s2[0]) + __uint_as_float(s2[1]);
}

__global__ __launch_bounds__(512, 2) void fused_ssm(
    const u16* __restrict__ x, const u16* __restrict__ Lre, const u16* __restrict__ Lim, const u16* __restrict__ Bre,
    const u16* __restrict__ Bim, const u16* __restrict__ Cre, const u16* __restrict__ Cim, const u16* __restrict__ ldtA,
    const u16* __restrict__ DA, const u16* __restrict__ u, const u16* __restrict__ lA, const u16* __restrict__ Aim,
    const u16* __restrict__ Cr, const u16* __restrict__ Ci, const u16* __restrict__ ldtB, const u16* __restrict__ DB,
    float* out, char* ws) {
#if defined(__HIP_DEVICE_COMPILE__)
  __shared__ __attribute__((aligned(16))) char smem_[163840];
  LAS char* smem = (LAS char*)smem_;
  STAMP(0);
  const int b = blockIdx.x, xcd = b & 7, j = b >> 3;
  const int tid = threadIdx.x, lane = tid & 63, w = __builtin_amdgcn_readfirstlane(tid >> 6);
  const int r = lane & 15, g = lane >> 4;
  const int odd = xcd & 1, mid = j >> 4;
  const int pi = mid ? (j - 16) + 2 * odd : (j >> 2), gi = mid ? 4 : (j & 3);
  const int pg = mid ? 4 : gi + 5 * odd, c = 4 * (xcd >> 1) + pi;
  const int wm = w & 3, wn = w >> 2;
  const int l3 = lane >> 3, ch = (lane & 7) ^ (l3 >> 1);
  const int rowx = 8 * (l3 >> 2) + 4 * (l3 & 1) + ((l3 >> 1) & 1), rowb = rowx ^ ((rowx ^ (2 * l3)) & -wn);
  const unsigned dvA = __umul24(rowb, 1152u) + ch * 16, dvB = dvA + 16 * 1152 + 64 - 2 * (dvA & 64);
  const unsigned rx_ = c * 128 + w * 32, rb_ = pg * 64 + (w & 1) * 32;
  const unsigned srow = (rx_ ^ ((rx_ ^ rb_) & -wn)) * 1152, stp = (2 - wn) * 1152;
  u64 pc0;
  asm volatile("s_getpc_b64 %0" : "=s"(pc0)
               : "v"(dvA), "v"(dvB), "s"(srow), "s"(stp), "s"(x), "s"(Lre), "s"(Lim), "s"(Bre), "s"(Bim), "s"(Cre), "s"(Cim), "s"(ldtA), "s"(DA),
                 "s"(u), "s"(lA), "s"(Aim), "s"(Cr), "s"(Ci), "s"(ldtB), "s"(DB), "s"(out), "s"(ws));

  if (__builtin_expect(j >= 18, 0)) {
  if (j < 27) {
    const int d0 = 72 * xcd + 8 * (j - 18), d = d0 + w;
    constexpr int UB = 8448, TB = UB + 4624, YB = TB + 4096, SL = YB + 1024;
    LAS char* sc = smem + w * SL;
    const unsigned qa = __builtin_nontemporal_load((const GAS unsigned*)(lA + d * 128 + 2 * lane)), qi = __builtin_nontemporal_load((const GAS unsigned*)(Aim + d * 128 + 2 * lane));
    const unsigned qr = __builtin_nontemporal_load((const GAS unsigned*)(Cr + d * 128 + 2 * lane)), qc = __builtin_nontemporal_load((const GAS unsigned*)(Ci + d * 128 + 2 * lane));
    const unsigned qd = ldtB[d], qs = DB[d];
    unsigned tch[6];
    const int R = (j - 18) * 128 + w * 16 + r;
    const u64 tb1 = (R < 512 ? (u64)x + (xcd >> 1) * 512 * 1152 : (R < 832 ? (u64)Bre + ((xcd & 1) * 256 - 512) * 1152 : (u64)Bim + ((xcd & 1) * 256 - 832) * 1152))
                    + R * 1152 + (g < 3 ? g : 2) * 128;
    const u64 tb2 = (R < 576 ? (u64)Cim : (u64)Cre - 576 * 1152) + R * 1152 + (g < 3 ? g : 2) * 128;
#define TOUCH1(k) do { FENCE(); tch[k] = *(const GAS unsigned*)(tb1 + (k) * 384); FENCE(); } while (0)
#define TOUCH2(k) do { FENCE(); tch[3 + k] = *(const GAS unsigned*)(tb2 + (k) * 384); FENCE(); } while (0)
    TOUCH1(0);
    const auto rsW = RSRC(ws, WS_END);
    const u64 tag = *(const CAS u64*)ws + TAG_STEP;
    asm volatile("" ::"s"((unsigned)tag), "s"((unsigned)(tag >> 32)));
    BARRIER();
    if (tid == 0) ASTORE((GAS u64*)(ws + WS_ZFLAG) + (b - 144) * 16, tag);
    v4u M5 = band(r, g, 6, 5), M3 = band(r, g, 11, 3), M9 = band(r, g, 4, 9), M7 = band(r, g, 5, 7), MP = band(r, g, 4, 7);
    M5 &= 0x3F803F80u; M3 &= 0x3F803F80u; M9 &= 0x3F803F80u; M7 &= 0x3F803F80u;
    asm volatile("" : "+v"(M5), "+v"(M3), "+v"(M9), "+v"(M7), "+v"(MP));
    if (lane < 33) *(LAS v4u*)(sc + UB + lane * 16) = v4u{0, 0, 0, 0};
#pragma unroll
    for (int i = 0; i < 4; ++i) *(LAS v4u*)(sc + TB + i * 1024 + lane * 16) = v4u{0, 0, 0, 0};
    if (w < 4) __builtin_amdgcn_s_setprio(1);
    const float dt = rbf(__builtin_amdgcn_exp2f(bflo(qd) * LOG2E));
    f32x2 l2 = bf2(pk2(exp2v(bf2(qa) * LOG2E))) * (-dt) * LOG2E, rv = redrev(bf2(qi) * dt);
    asm volatile("" : "+v"(l2), "+v"(rv));
    TOUCH1(1);
    STAMP(1);
    f32x4 kacc[3] = {};
    {
      const c2 w1 = cpow(l2, rv, sp(1.f)), v8 = cpow(l2, -rv, sp(8.f)), Q = cpow(l2, rv, sp(128.f));
      LAS char* tw = sc + lane * 8;
      LAS char* tr = sc + r * 528 + g * 16;
#define PUTG(row, z) (*(LAS v2u*)(tw + (row) * 528) = v2u{pk(z.re[0], z.im[0]), pk(z.re[1], z.im[1])})
      *(LAS v2u*)(tw) = v2u{0x3F80u, 0x3F80u};
      c2 f = v8;
      PUTG(1, f);
#pragma unroll
      for (int i = 2; i < 16; ++i) { f = cmul(f, v8); PUTG(i, f); }
      TOUCH1(2);
      v4u fF[8];
#pragma unroll
      for (int ks = 0; ks < 8; ++ks) fF[ks] = lds128(tr + ks * 64);
      c2 Rw[8];
      Rw[0] = {bf2(qr) * (2.f * dt), bf2(qc) * (2.f * dt)};
#pragma unroll
      for (int i = 1; i < 8; ++i) Rw[i] = cmul(Rw[i - 1], w1);
#pragma unroll
      for (int i = 0; i < 8; ++i) { PUTG(i, Rw[i]); Rw[i] = cmul(Rw[i], Q); PUTG(8 + i, Rw[i]); }
#pragma unroll
      for (int ks = 0; ks < 8; ++ks) kacc[0] = mfma(lds128(tr + ks * 64), fF[ks], kacc[0]);
      STAMP(2);
      TOUCH2(0);
#pragma unroll
      for (int s = 0; s < 2; ++s)
#pragma unroll
        for (int i = 0; i < 4; ++i) { Rw[2 * i] = cmul(Rw[2 * i], Q); PUTG(4 * s + i, Rw[2 * i]); }
#pragma unroll
      for (int s = 0; s < 4; ++s)
#pragma unroll
        for (int i = 0; i < 2; ++i) { Rw[4 * i] = cmul(Rw[4 * i], Q); PUTG(8 + 2 * s + i, Rw[4 * i]); }
#pragma unroll
      for (int ks = 0; ks < 8; ++ks) kacc[1] = mfma(lds128(tr + ks * 64), fF[ks], kacc[1]);
      TOUCH2(1);
#pragma unroll
      for (int s = 0; s < 9; ++s) { Rw[0] = cmul(Rw[0], Q); PUTG(s, Rw[0]); }
#pragma unroll
      for (int ks = 0; ks < 8; ++ks) kacc[2] = mfma(lds128(tr + ks * 64), fF[ks], kacc[2]);
    }
    TOUCH2(2);
    STAMP(3);
    {
      const GAS u64* fp = (const GAS u64*)(ws + WS_TFLAG) + ((d >> 6) * 4 + (lane & 3)) * 16;
#pragma unroll 1
      for (unsigned sp_ = 0; sp_ < SPIN_CAP; ++sp_) {
        FENCE();
        if (__all(ALOAD(fp) == tag)) break;
        __builtin_amdgcn_s_sleep(8);
      }
      FENCE();
      STAMP(6);
#pragma unroll
      for (int i = 0; i < 4; ++i) __builtin_amdgcn_raw_ptr_buffer_load_lds(rsW, sc + UB + 528 + i * 1024, 16, lane * 16, WS_UT + d * UTS + i * 1024, 0, 16);
      FENCE();
    }
    constexpr int KD = TB, KDS = 1216, T1 = TB + 2432, E2 = TB + 2704, O2 = E2 + 256, E3 = E2 + 512, T3S = 208;
    v4u T[17], TA2[2][3], P;
    {
      const unsigned k1a = pk(kacc[1][0], kacc[1][1]), k1b = pk(kacc[1][2], kacc[1][3]);
      const unsigned k2a = pk(kacc[2][0], kacc[2][1]), k2b = pk(kacc[2][2], kacc[2][3]);
      const unsigned v512 = (__builtin_amdgcn_readlane(k1a, 32) & 0xFFFFu) * 0x10001u, v1024 = (__builtin_amdgcn_readlane(k2a, 0) & 0xFFFFu) * 0x10001u;
      if (g < 2) {
        *(LAS v2u*)(sc + T1 + (4 * r + 64 * g) * 2) = v2u{k1a, k1b};
        LAS char* t3 = sc + E3 + (r & 1) * (2 * T3S - 2) + (65 - 32 * g - (r >> 1)) * 2;
        *(LAS u16*)(t3) = (u16)k2a; *(LAS u16*)(t3 + T3S + 2) = (u16)k2a;
        *(LAS u16*)(t3 - 16) = (u16)(k2a >> 16); *(LAS u16*)(t3 - 16 + T3S + 2) = (u16)(k2a >> 16);
        *(LAS u16*)(t3 - 32) = (u16)k2b; *(LAS u16*)(t3 - 32 + T3S + 2) = (u16)k2b;
        *(LAS u16*)(t3 - 48) = (u16)(k2b >> 16); *(LAS u16*)(t3 - 48 + T3S + 2) = (u16)(k2b >> 16);
      } else {
        LAS char* t2 = sc + E2 + (96 - r - 32 * (g - 2)) * 2;
        *(LAS u16*)(t2) = (u16)k1a; *(LAS u16*)(t2 + 256 - 2) = (u16)(k1a >> 16);
        *(LAS u16*)(t2 - 32) = (u16)k1b; *(LAS u16*)(t2 - 32 + 256 - 2) = (u16)(k1b >> 16);
        if (lane == 32) { *(LAS u16*)(sc + E3 + 2) = (u16)k2a; *(LAS u16*)(sc + E3 + T3S + 4) = (u16)k2a; }
      }
      FENCE();
      if (lane == 0) {
        *(LAS unsigned*)(sc + T1 + 256) = v512;
        *(LAS u16*)(sc + E2 + 192) = 0; *(LAS u16*)(sc + E3 + 130) = 0; *(LAS u16*)(sc + E3 + T3S + 132) = 0;
        *(LAS v2u*)(sc + KD + 64) = v2u{v512, v512};
        *(LAS v2u*)(sc + KD + KDS + 64) = v2u{v512 << 16, v512};
      }
      FENCE();
      const unsigned a1 = *(LAS unsigned*)(sc + T1 + lane * 4), b1 = *(LAS unsigned*)(sc + T1 + lane * 4 + 4);
      const unsigned e0 = perm(a1, a1, 0x01000100u), m10 = perm(a1, a1, 0x01000302u), m21 = perm(a1, b1, 0x07060100u);
      *(LAS v2u*)(sc + KD + KDS + (288 - 4 * lane) * 2) = v2u{m21, e0};
      *(LAS v2u*)(sc + KD + (288 - 4 * lane) * 2) = v2u{m10, e0};
      FENCE();
      const int q0 = 8 * r + 4 * (g & 1) + 128 * (g >> 1);
      const unsigned v32 = pk(kacc[0][3], kacc[0][2]), v10 = pk(kacc[0][1], kacc[0][0]), v21 = pk(kacc[0][2], kacc[0][1]);
      *(LAS v2u*)(sc + KD + KDS + (544 - q0) * 2) = v2u{v32, v10};
      *(LAS u16*)(sc + KD + (543 - q0) * 2) = (u16)v32;
      FENCE();
      *(LAS unsigned*)(sc + KD + (544 - q0) * 2) = v21;
      FENCE();
      *(LAS u16*)(sc + KD + (546 - q0) * 2) = (u16)(v10 >> 16);
      FENCE();
      P = MP & v1024;
#define LD4(p) v4u{((LAS unsigned*)(p))[0], ((LAS unsigned*)(p))[1], ((LAS unsigned*)(p))[2], ((LAS unsigned*)(p))[3]}
      LAS char* pT = sc + KD + (r & 1) * KDS + (18 - (r & 14) + 8 * g) * 2;
#pragma unroll
      for (int t = 0; t < 17; ++t) T[t] = LD4(pT + (16 - t) * 64);
      LAS char* pA = sc + E2 + (30 - 2 * r + 8 * g) * 2;
#pragma unroll
      for (int dl = 0; dl < 3; ++dl) { TA2[0][dl] = LD4(pA + (2 - dl) * 64); TA2[1][dl] = LD4(pA + 256 + (2 - dl) * 64); }
    }
    WAITV(0);
    STAMP(4);
    {
      f32x4 ya[8] = {};
      f32x4 d9[2], d7[2];
      LAS char* pU = sc + UB + (16 * r + 8 * g) * 2;
      LAS char* p3 = sc + E3 + (r & 1) * T3S + (16 - (r & 14) + 4 * g) * 2;
#define UFR(sg) lds128(pU + (248 + 32 * (sg)) * 2)
#define TAP3(par, ep) v4u{((LAS unsigned*)(p3 + (par) * 2 * T3S + (7 - (ep)) * 32))[0], ((LAS unsigned*)(p3 + (par) * 2 * T3S + (7 - (ep)) * 32))[1], \
                          ((LAS unsigned*)(p3 + (par) * 2 * T3S + (7 - (ep)) * 32))[8], ((LAS unsigned*)(p3 + (par) * 2 * T3S + (7 - (ep)) * 32))[9]}
      const f32x2 dsk = sp(bflo(qs));
      LAS char* sw_ = sc + YB + (4 * r + g) * 8;
      v4u TA3[2][4];
      v4u U[4], ov;
#pragma unroll
      for (int i = 0; i < 3; ++i) U[i] = UFR(-7 + i);
#pragma unroll
      for (int B = 0; B < 8; ++B) {
        v4u W0, Wp;
        if (B <= 5) W0 = lds128(pU + (240 + 256 * B) * 2);
        if (B >= 3) Wp = lds128(pU + (256 + 256 * (B - 4)) * 2);
        const v2u u4 = lds64(sc + UB + (264 + 256 * B + 16 * r + 4 * g) * 2);
        v4u W1;
#pragma unroll
        for (int i = 7; i >= 0; --i) {
          const int sg = 8 * B - i, ix = sg + 7;
          if (sg + 3 <= 56) U[(ix + 3) & 3] = UFR(sg + 3);
          const v4u Uc = U[ix & 3];
          ya[B] = mfma(T[i], Uc, ya[B]);
          if (B + 1 <= 7) ya[B + 1] = mfma(T[i + 8], Uc, ya[B + 1]);
          if (i == 0) { if (B + 2 <= 7) ya[B + 2] = mfma(T[16], Uc, ya[B + 2]); W1 = Uc; }
          if (i == 7 && B >= 3) ya[B] = mfma(P, Wp, ya[B]);
          SCHED();
          if (B == 1 && i >= 4) { TA3[0][i - 4] = TAP3(0, i); TA3[1][i - 4] = TAP3(1, i); }
          if (i == 6 && B > 0 && !(B & 1)) __builtin_amdgcn_raw_buffer_store_b128(ov, rsW, lane * 16, WS_YT + d * UTS + (B - 2) * 512, 16);
          SCHED();
        }
        f32x4 d50, d51, d30, d31;
        const f32x4 z4 = {0.f, 0.f, 0.f, 0.f};
        if (B <= 5) { d50 = mfma(W0, M5, z4); d51 = mfma(W1, M5, z4); d30 = mfma(W0, M3, z4); d31 = mfma(W1, M3, z4); }
        if (B <= 3) { d9[B & 1] = mfma(W0, M9, z4); d7[B & 1] = mfma(W1, M7, z4); }
        SCHED();
        {
          const f32x2 o01 = fma2(bf2(u4[0]), dsk, ya[B].xy), o23 = fma2(bf2(u4[1]), dsk, ya[B].zw);
          *(LAS v2u*)(sw_ + (B & 1) * 512) = v2u{pk2(o01), pk2(o23)};
          if (B & 1) { FENCE(); ov = lds128(sc + YB + lane * 16); FENCE(); }
        }
        if (B <= 5) {
          const v4u X5 = {pk(d50[0], d51[0]), pk(d50[1], d51[1]), pk(d50[2], d51[2]), pk(d50[3], d51[3])};
          const v4u X3 = {pk(d30[0], d31[0]), pk(d30[1], d31[1]), pk(d30[2], d31[2]), pk(d30[3], d31[3])};
#pragma unroll
          for (int dl = 0; dl < 3; ++dl)
            if (B + 2 + dl <= 7) { ya[B + 2 + dl] = mfma(X5, TA2[0][dl], ya[B + 2 + dl]); ya[B + 2 + dl] = mfma(X3, TA2[1][dl], ya[B + 2 + dl]); }
        }
        if (B == 1 || B == 3) {
          const v4u X9 = {pk2(d9[0].xy), pk2(d9[0].zw), pk2(d9[1].xy), pk2(d9[1].zw)};
          const v4u X7 = {pk2(d7[0].xy), pk2(d7[0].zw), pk2(d7[1].xy), pk2(d7[1].zw)};
#pragma unroll
          for (int q = 4 + (B - 1); q <= 7; ++q) { ya[q] = mfma(X9, TA3[0][q - (B - 1) - 4], ya[q]); ya[q] = mfma(X7, TA3[1][q - (B - 1) - 4], ya[q]); }
        }
        SCHED();
      }
      __builtin_amdgcn_raw_buffer_store_b128(ov, rsW, lane * 16, WS_YT + d * UTS + 6 * 512, 16);
      STAMP(5);
    }
    WAITV(0);
    BARRIER();
    if (tid == 0) ASTORE((GAS u64*)(ws + WS_YFLAG) + (d0 >> 3) * 16, tag);
    asm volatile("" ::"v"(tch[0]), "v"(tch[1]), "v"(tch[2]), "v"(tch[3]), "v"(tch[4]), "v"(tch[5]));
    STAMP_END();
  } else if (b != 255 || w != 0) {
    unsigned v = 0;
    const int hw = (b - 216) * 8 + w;
    if (j == 27 && tid < CODE_LINES) v = *(const GAS unsigned*)((pc0 & ~127ull) + tid * 128);
    STAMP(1);
    if (hw < 288) {
      const int rb = hw & 31, lc = hw >> 5;
      const auto rsU = RSRC(u, 2048 * 1152);
      LAS char* sp_ = smem + w * 8192;
      const unsigned uvo = (rb * 64 + (lane >> 3) * 8) * 1152 + lc * 128 + (((lane & 7) ^ ((lane >> 3) & 6)) * 16);
#pragma unroll
      for (int i = 0; i < 8; ++i) __builtin_amdgcn_raw_ptr_buffer_load_lds(rsU, sp_ + i * 1024, 16, uvo, i * 1152, 0, TAUX);
      const u64 tag = *(const CAS u64*)ws + TAG_STEP;
      const auto rsW = RSRC(ws, WS_END);
      WAITV(0);
      STAMP(2);
      v4u rw[8];
#pragma unroll
      for (int i = 0; i < 8; ++i) rw[i] = lds128(sp_ + i * 1024 + (lane & 7) * 128 + (((lane >> 3) ^ (lane & 6)) * 16));
      const unsigned uo = WS_UT + (lc * 64 + (lane >> 3) * 8) * UTS + rb * 128 + (lane & 7) * 16;
#pragma unroll
      for (int e = 0; e < 8; ++e) {
        v4u o;
#pragma unroll
        for (int k = 0; k < 4; ++k) o[k] = perm(rw[2 * k + 1][e >> 1], rw[2 * k][e >> 1], (e & 1) ? 0x07060302u : 0x05040100u);
        __builtin_amdgcn_raw_buffer_store_b128(o, rsW, uo + e * UTS, 0, 16);
      }
      WAITV(0);
      BARRIER();
      if (tid == 0) ASTORE((GAS u64*)(ws + WS_TFLAG) + (b - 216) * 16, tag);
      FENCE();
      STAMP(3);
      if (w == 0) {
        const GAS u64* fp = (const GAS u64*)(ws + WS_YFLAG) + (lc * 8 + (lane & 7)) * 16;
        __builtin_amdgcn_s_sleep(YDELAY);
#pragma unroll 1
        for (unsigned sp_ = 0; sp_ < SPIN_CAP; ++sp_) {
          FENCE();
          if (__all(ALOAD(fp) == tag)) break;
          __builtin_amdgcn_s_sleep(6);
        }
      }
      BARRIER();
      STAMP(4);
      const unsigned yvo = (lane >> 3) * UTS + (((lane & 7) ^ (lane >> 5)) * 16);
#pragma unroll
      for (int i = 0; i < 8; ++i)
        __builtin_amdgcn_raw_ptr_buffer_load_lds(rsW, sp_ + i * 1024, 16, yvo ^ (((2 * i) & 7) * 16), WS_YT + (lc * 64 + 8 * i) * UTS + rb * 128, 0, 16);
      WAITV(0);
      STAMP(5);
#pragma unroll
      for (int e = 0; e < 4; ++e)
#pragma unroll
        for (int hh = 0; hh < 2; ++hh) rw[2 * e + hh] = lds128(sp_ + (4 * r + e) * 128 + (((2 * g + hh) ^ (r & 7)) * 16));
      const auto rsO = RSRC(out, 2048 * 1152 * 4);
      const unsigned oo = ((rb * 64 + 16 * g) * 1152 + 576 + lc * 64 + 4 * r) * 4;
#pragma unroll
      for (int i = 0; i < 16; ++i) {
        v4u o;
#pragma unroll
        for (int e = 0; e < 4; ++e) { const unsigned q_ = rw[2 * e + (i >> 3)][(i >> 1) & 3]; o[e] = (i & 1) ? (q_ & 0xFFFF0000u) : (q_ << 16); }
        __builtin_amdgcn_raw_buffer_store_b128(o, rsO, oo + i * 4608, 0, AUXB);
      }
    }
    asm volatile("" ::"v"(v));
    STAMP_END();
  } else {
    const u64 tag = *(const CAS u64*)ws + TAG_STEP;
    const GAS u64* fl = (const GAS u64*)(ws + WS_FLAG);
#pragma unroll 1
    for (unsigned sp_ = 0; sp_ < SPIN_CAP; ++sp_) {
      __builtin_amdgcn_s_sleep(32);
      FENCE();
      bool ok = true;
#pragma unroll
      for (int i = 0; i < 4; ++i) {
        const int f = i * 64 + lane;
        ok &= ALOAD(fl + (f < 252 ? f : 0) * 16) == tag;
      }
      if (__all(ok)) break;
    }
    if (lane == 0) ASTORE((GAS u64*)ws, tag);
  }
  return;
  }
  {
    const int p0 = 64 * pg + 32 * wn + 2 * r;
    const auto rsS = RSRC((u64)(w < 4 ? x : (w < 6 ? Bre : Bim)) + srow, 32 * 1152);
#define SLOT(s) (((8 - (s)) % 3) * 32768)
#define SPIECE(s, i) __builtin_amdgcn_raw_ptr_buffer_load_lds(rsS, smem + SLOT(s) + w * 4096 + (i) * 1024, 16, ((i) & 1) ? dvB : dvA, (s) * 128 + ((i) >> 1) * stp, 0, 0)
    SPIECE(0, 0); SPIECE(0, 1); SPIECE(0, 2); SPIECE(0, 3);
    asm volatile("s_barrier" ::: "memory");
    SPIECE(1, 0); SPIECE(1, 1); SPIECE(1, 2); SPIECE(1, 3);
    FENCE();
    unsigned pLr = *(const GAS unsigned*)(Lre + p0), pLi = *(const GAS unsigned*)(Lim + p0), pDt = *(const GAS unsigned*)(ldtA + p0);
    FENCE();
    u64 tag = *(const GAS u64*)ws;
    FENCE();
    const auto rsW = RSRC(ws, WS_END);
    const int c2_ = 2 * xcd + (j >= 9), n = j >= 9 ? j - 9 : j;
    const auto rsCre = RSRC(Cre + 64 * n * 576, 64 * 1152);
    const auto rsCim = RSRC(Cim + 64 * n * 576, 64 * 1152);
    const unsigned cvo = __umul24(32 * (w & 1) + 4 * l3 + (w >> 1), 1152u) + (((lane & 7) ^ ((4 * w + (l3 >> 1)) & 7)) * 16);
#define CPIECE(q) __builtin_amdgcn_raw_ptr_buffer_load_lds((q) < 9 ? rsCre : rsCim, smem + 16384 + (q) * 8192 + w * 1024, 16, cvo, ((q) % 9) * 128, 0, 0)
    c2 lam, l4, l8, l16, l32, coef, lgc, wt, wE[2];
    f32x2 lr, li, l2, rv;
    unsigned eaddr, waddr, hoff;

    const unsigned fo = r * 128 + ((g ^ (r >> 1)) * 16);
    LAS char* pa0 = smem + wm * 4096 + fo;
    LAS char* pa1 = smem + wm * 4096 + (fo ^ 64);
    LAS char* pb0 = smem + 16384 + wn * 4096 + fo;
    LAS char* pb1 = smem + 16384 + wn * 4096 + (fo ^ 64);
    f32x4 acc[2][2][2] = {};
    v4u fr[2][12];
    STAMP(1);
#pragma unroll
    for (int s = 0; s < 10; ++s) {
      if (s == 0) WAITV(8); else if (s <= 8) WAITV(4);
      BARRIER();
      if (s == 1) { asm volatile("" : "+v"(tag)); tag += TAG_STEP; asm volatile("" : "+v"(tag)); }
      if (s == 0) STAMP(2);
      if (s == 3) STAMP(11);
      if (s == 6) STAMP(12);
      if (s == 9) STAMP(13);
#pragma unroll
      for (int i = 0; i < 16; ++i) {
        if (s < 9 && i < 12) {
          const int kk = i / 6, e = i % 6;
          LAS char* pa = (kk ? pa1 : pa0) + SLOT(s);
          LAS char* pb = (kk ? pb1 : pb0) + SLOT(s);
          fr[s & 1][i] = e == 0 ? lds128(pa) : e == 5 ? lds128(pa + 2048) : lds128(pb + ((e - 1) >> 1) * 8192 + ((e - 1) & 1) * 2048);
        }
        if (s >= 1) {
          const int kk = i >> 3, m = s == 9 && kk ? (i & 1) : (i >> 2) & 1, e = s == 9 && kk ? (((i >> 1) & 1) * 2 + ((i >> 2) & 1)) : i & 3;
          acc[m][e >> 1][e & 1] = mfma(fr[(s - 1) & 1][6 * kk + 5 * m], fr[(s - 1) & 1][6 * kk + 1 + e], acc[m][e >> 1][e & 1]);
        }
        if ((i & 3) == 3) {
          if (s + 2 < 9) SPIECE(s + 2, i >> 2);
          if (s == 7) CPIECE(10 + (i >> 2));
          if (s == 8) CPIECE(14 + (i >> 2));
        }
        if (s < 9 || i < 12) SCHED();
      }
      if (s == 0) {
    asm volatile("" : "+v"(pLr), "+v"(pLi), "+v"(pDt));
    lr = bf2(pLr); li = bf2(pLi);
    {
      const f32x2 dt = bf2(pk2(exp2v(bf2(pDt) * LOG2E)));
      l2 = dt * lr * LOG2E; rv = redrev(dt * li);
      lam = cpow(l2, rv, sp(1.f));
      l4 = csqr(csqr(lam)); l8 = csqr(l4); l16 = csqr(l8); l32 = csqr(l16);
      const c2 wB = cpow(l2, rv, sp((float)(120 - 32 * wm - 8 * g))), wA = cmul(wB, l4);
      wE[0] = {f32x2{wA.re[0], wB.re[0]}, f32x2{wA.im[0], wB.im[0]}};
      wE[1] = {f32x2{wA.re[1], wB.re[1]}, f32x2{wA.im[1], wB.im[1]}};
    }
    PIN2(lam); PIN2(l4); PIN2(l8); PIN2(l16); PIN2(l32); PIN2(wE[0]); PIN2(wE[1]);
        const f32x2 den = lr * lr + li * li, inv = f32x2{__builtin_amdgcn_rcpf(den[0]), __builtin_amdgcn_rcpf(den[1])} * 2.f, nr = lam.re - 1.f;
        coef = {(nr * lr + lam.im * li) * inv, (lam.im * lr - nr * li) * inv};
        const c2 one = {sp(1.f), sp(0.f)};
        lgc = cmul(coef, cmul((g & 1) ? l8 : one, (g & 2) ? l16 : one));
        unsigned ln = lane;
        asm volatile("" : "+v"(ln));
        eaddr = WS_E + (c * 576 + 64 * pg + 32 * wn + 2 * (ln & 15) + (ln >> 4)) * 16;
        waddr = (16 * wn + (ln & 15)) * 16;
        hoff = WS_H + (4 * c + wm) * 2 * 36864 + (2 * pg + wn) * 2048 + ln * 16;
        PIN2(coef); PIN2(lgc);
        asm volatile("" : "+v"(eaddr), "+v"(waddr), "+v"(hoff));
        SCHED();
      }
    }
    STAMP(3);
    const unsigned tagA = (unsigned)tag, tagB = (unsigned)(tag >> 32);
#define BSTEP(t, s) c2{f32x2{acc[(s) >> 1][0][t][2 * ((s) & 1)], acc[(s) >> 1][0][t][2 * ((s) & 1) + 1]}, f32x2{acc[(s) >> 1][1][t][2 * ((s) & 1)], acc[(s) >> 1][1][t][2 * ((s) & 1) + 1]}}
    LAS char* sW = smem;
    LAS char* sH = smem + 2048;
    LAS char* sS = smem + 4096;
    c2 e2[2];
#pragma unroll
    for (int t = 0; t < 2; ++t) {
      e2[t] = BSTEP(t, 0);
#pragma unroll
      for (int s = 1; s < 4; ++s) e2[t] = cfma(el(lam, t), e2[t], BSTEP(t, s));
    }
    {
      const c2 u0 = cmul(wE[0], e2[0]), u1 = cmul(wE[1], e2[1]);
      *(LAS float*)(sS + wm * 512 + waddr + g * 4) = rsum4(u0.re[0] + u0.re[1], u0.im[0] + u0.im[1], u1.re[0] + u1.re[1], u1.im[0] + u1.im[1]);
    }
    const bool pub = w == 0 || w == 5;
    if (pub) __builtin_amdgcn_s_setprio(2);
    BARRIER();
    if (pub) {
      LAS char* q_ = sS + waddr + (g & 1) * 8;
      const f32x2 E = (*(LAS f32x2*)q_ + *(LAS f32x2*)(q_ + 512)) + (*(LAS f32x2*)(q_ + 1024) + *(LAS f32x2*)(q_ + 1536));
      if (g < 2) __builtin_amdgcn_raw_buffer_store_b128(v4u{__float_as_uint(E[0]), tagA, __float_as_uint(E[1]), tagB}, rsW, eaddr, 0, 16);
      __builtin_amdgcn_s_setprio(0);
    }
    FENCE();
    STAMP(4);
    CPIECE(9); CPIECE(0); CPIECE(1); CPIECE(2); CPIECE(3);
    FENCE();
    c2 eA, ea, ea2, pw;
    {
      eA = {f32x2{e2[0].re[0], e2[1].re[0]}, f32x2{e2[0].im[0], e2[1].im[0]}};
      const c2 e = cfma(l4, eA, c2{f32x2{e2[0].re[1], e2[1].re[1]}, f32x2{e2[0].im[1], e2[1].im[1]}});
      c2 hi;
      ea.re = swap16(e.re, hi.re); ea.im = swap16(e.im, hi.im);
      const c2 A = cfma(l8, ea, hi);
      ea2.re = swap32(A.re, hi.re); ea2.im = swap32(A.im, hi.im);
      const c2 W = cfma(l16, ea2, hi);
      *(LAS f32x4*)(sW + wm * 512 + waddr) = f32x4{W.re[0], W.re[1], W.im[0], W.im[1]};
    }
    c2 bs[2][4];
#pragma unroll
    for (int t = 0; t < 2; ++t)
#pragma unroll
      for (int s = 0; s < 4; ++s) { bs[t][s] = cmulj(el(coef, t), BSTEP(t, s)); PIN2(bs[t][s]); }
    unsigned ln2 = lane;
    asm volatile("" : "+v"(ln2));
    const unsigned paddr = WS_E + ((4 * wm + (4 * wm + (int)(ln2 >> 4) < c ? (ln2 >> 4) : 0)) * 576 + 64 * pg + 32 * wn + 2 * (ln2 & 15)) * 16;
    {
      asm volatile("" : "+v"(l2), "+v"(rv));
      const int np = c - 1 - 4 * wm - g;
      wt = cpow(l2, rv, sp((float)(128 * (np > 0 ? np : 0))));
      const float mk = np >= 0 ? 1.f : 0.f;
      wt = {wt.re * mk, wt.im * mk};
      PIN2(wt);
    }
    BARRIER();
    v4u e0 = {0u, 0u, 0u, 0u}, e1 = e0;
    if (4 * wm < c) { e0 = __builtin_amdgcn_raw_buffer_load_b128(rsW, paddr, 0, 16); e1 = __builtin_amdgcn_raw_buffer_load_b128(rsW, paddr + 16, 0, 16); }
    FENCE();
    {
      f32x4 wv[3];
#pragma unroll
      for (int i = 0; i < 3; ++i) wv[i] = *(LAS f32x4*)(sW + i * 512 + waddr);
      const c2 s1 = {wv[0].xy, wv[0].zw};
      const c2 s2 = cfma(l32, s1, c2{wv[1].xy, wv[1].zw});
      const c2 s3 = cfma(l32, s2, c2{wv[2].xy, wv[2].zw});
      pw.re = wm == 0 ? sp(0.f) : wm == 1 ? s1.re : wm == 2 ? s2.re : s3.re;
      pw.im = wm == 0 ? sp(0.f) : wm == 1 ? s1.im : wm == 2 ? s2.im : s3.im;
    }
    c2 pf2[2], lp2[2];
    unsigned xoff = (128 * c2_ + 32 * (w >> 1) + 4 * (w & 1) + 8 * (ln2 >> 4)) * 1152 + 128 * n + 8 * (ln2 & 15);
    asm volatile("" : "+v"(xoff));
    {
      c2 lw = lgc;
      if (wm & 1) lw = cmul(lw, l32);
      if (wm & 2) lw = cmul(lw, csqr(l32));
      const c2 lwB = cmul(l4, lw);
      lp2[0] = {f32x2{lw.re[0], lwB.re[0]}, f32x2{-lw.im[0], -lwB.im[0]}};
      lp2[1] = {f32x2{lw.re[1], lwB.re[1]}, f32x2{-lw.im[1], -lwB.im[1]}};
      PIN2(lp2[0]); PIN2(lp2[1]);
      const c2 z = {sp(0.f), sp(0.f)};
      const c2 o = (g & 2) ? ea2 : z;
      const c2 pin = (g & 1) ? cfma(l8, o, ea) : o;
      const c2 pfA = cfma(lgc, pw, cmul(coef, pin));
      const c2 pfB = cfma(l4, pfA, cmul(coef, eA));
#pragma unroll
      for (int t = 0; t < 2; ++t) {
        pf2[t] = {f32x2{pfA.re[t], pfB.re[t]}, f32x2{-pfA.im[t], -pfB.im[t]}};
        PIN2(pf2[t]);
      }
    }
    FENCE();
    {
      float hsum = 0.f;
      if (4 * wm < c) {
#define EOK() __all((((e0[1] ^ tagA) | (e0[3] ^ tagB)) | ((e1[1] ^ tagA) | (e1[3] ^ tagB))) == 0)
        if (!EOK()) {
#pragma unroll 1
          for (unsigned sp_ = 0; sp_ < SPIN_CAP; ++sp_) {
            FENCE();
            e0 = __builtin_amdgcn_raw_buffer_load_b128(rsW, paddr, 0, 16);
            e1 = __builtin_amdgcn_raw_buffer_load_b128(rsW, paddr + 16, 0, 16);
            if (EOK()) break;
          }
        }
        const c2 hp = cmulj(wt, c2{f32x2{__uint_as_float(e0[0]), __uint_as_float(e1[0])}, f32x2{__uint_as_float(e0[2]), __uint_as_float(e1[2])}});
        hsum = rsum4(hp.re[0], hp.re[1], hp.im[0], hp.im[1]);
      }
      *(LAS float*)(sH + wm * 512 + waddr + g * 4) = hsum;
    }
    STAMP(5);
    BARRIER();
    {
      const f32x4 hs = (*(LAS f32x4*)(sH + waddr) + *(LAS f32x4*)(sH + 512 + waddr)) + (*(LAS f32x4*)(sH + 1024 + waddr) + *(LAS f32x4*)(sH + 1536 + waddr));
      const c2 H = {hs.xy, hs.zw}, lamj = cj(lam);
      c2 h0 = cfma(lp2[0], el(H, 0), pf2[0]), h1 = cfma(lp2[1], el(H, 1), pf2[1]);
      unsigned dv[4][4];
#pragma unroll
      for (int s = 0; s < 4; ++s) {
        h0 = cfma(el(lamj, 0), h0, bs[0][s]);
        h1 = cfma(el(lamj, 1), h1, bs[1][s]);
        dv[0][s] = pk(h0.re[0], h1.re[0]); dv[2][s] = pk(h0.re[1], h1.re[1]);
        dv[1][s] = pk(h0.im[0], h1.im[0]); dv[3][s] = pk(h0.im[1], h1.im[1]);
      }
      LAS char* tb_ = smem + 8192 + w * 1024;
      LAS char* tw_ = tb_ + ((r >> 2) * 16 + 4 * g) * 16 + (r & 3) * 4;
      LAS char* tr_ = tb_ + (lane ^ g) * 16;
      v4u o[4];
#pragma unroll
      for (int e = 0; e < 4; ++e) {
#pragma unroll
        for (int s = 0; s < 4; ++s) *(LAS unsigned*)(tw_ + ((s ^ (r >> 2)) * 16)) = dv[e][s];
        FENCE();
        o[e] = lds128(tr_);
        FENCE();
      }
#pragma unroll
      for (int e = 0; e < 4; ++e) __builtin_amdgcn_raw_buffer_store_b128(o[e], rsW, hoff + (e >> 1) * 36864 + (e & 1) * 1024, 0, 16);
    }
    STAMP(6);
    f32x2 sk[4][2];
    {
      v2u xs[4];
      const auto rsX = RSRC(x, 2048 * 1152);
#pragma unroll
      for (int i = 0; i < 4; ++i) xs[i] = __builtin_amdgcn_raw_buffer_load_b64(rsX, xoff, i * 1152, 0);
      const v2u dsk = *(const GAS v2u*)(DA + 64 * n + 4 * r);
      FENCE();
      CPIECE(4); CPIECE(5); CPIECE(6); CPIECE(7); CPIECE(8);
      WAITV(10);
      BARRIER();
      STAMP(7);
      if (w == 0) {
        if (lane == 0) ASTORE((GAS u64*)(ws + WS_FLAG) + (c * 9 + pg) * 16, tag);
        const GAS u64* fp = (const GAS u64*)(ws + WS_FLAG) + (c2_ * 9 + (lane < 9 ? lane : 0)) * 16;
#pragma unroll 1
        for (unsigned sp_ = 0; sp_ < SPIN_CAP; ++sp_) {
          FENCE();
          if (__all(ALOAD(fp) == tag)) break;
          __builtin_amdgcn_s_sleep(1);
        }
      }
      const f32x2 d01 = bf2(dsk[0]), d23 = bf2(dsk[1]);
#pragma unroll
      for (int i = 0; i < 4; ++i) {
        sk[i][0] = bf2(pk2(bf2(xs[i][0]) * d01)); sk[i][1] = bf2(pk2(bf2(xs[i][1]) * d23));
        asm volatile("" : "+v"(sk[i][0]), "+v"(sk[i][1]));
      }
    }
    WAITV(0);
    BARRIER();
    STAMP(8);
    {
      constexpr int HD = 8;
      const unsigned hsrc = WS_H + (c2_ * 8 + w) * 36864;
      LAS char* pc0_ = smem + 16384 + fo;
      LAS char* pc1_ = smem + 16384 + (fo ^ 64);
      f32x4 a[4] = {};
#define CFRAG(k1, jt) lds128(((((k1) >> 1) & 1) ? pc1_ : pc0_) + (((k1) & 1) * 9 + ((k1) >> 2)) * 8192 + (jt) * 2048)
      v4u hf[HD], cf[2][4];
#pragma unroll
      for (int i = 0; i < HD; ++i) hf[i] = __builtin_amdgcn_raw_buffer_load_b128(rsW, lane * 16, hsrc + i * 1024, 16);
#pragma unroll
      for (int jt = 0; jt < 4; ++jt) cf[0][jt] = CFRAG(0, jt);
#pragma unroll
      for (int ks = 0; ks < 36; ++ks) {
        const v4u hc = hf[ks % HD];
#pragma unroll
        for (int jt = 0; jt < 4; ++jt) {
          if (ks + 1 < 36) cf[(ks + 1) & 1][jt] = CFRAG(ks + 1, jt);
          a[jt] = mfma(hc, cf[ks & 1][jt], a[jt]);
          SCHED();
        }
        if (ks + HD < 36) hf[ks % HD] = __builtin_amdgcn_raw_buffer_load_b128(rsW, lane * 16, hsrc + (ks + HD) * 1024, 16);
        SCHED();
        if (ks == 0) STAMP(9);
      }
      STAMP(10);
      const auto rsO = RSRC(out, 2048 * 1152 * 4);
      const unsigned oo = (xoff - 128 * n - 8 * r) * 4 + 256 * n + 16 * r;
#pragma unroll
      for (int i = 0; i < 4; ++i)
        __builtin_amdgcn_raw_buffer_store_b128(__builtin_bit_cast(v4u, f32x4{a[0][i] + sk[i][0][0], a[1][i] + sk[i][0][1], a[2][i] + sk[i][1][0], a[3][i] + sk[i][1][1]}),
                                               rsO, oo + i * 4608, 0, AUXA);
    }
    STAMP_END();
  }
#endif
}

extern "C" void kernel_launch(void* const* d_in, const int* in_sizes, int n_in, void* d_out, int out_size, void* d_ws,
                              size_t ws_size, hipStream_t stream) {
  fused_ssm<<<256, 512, 0, stream>>>((const u16*)d_in[0], (const u16*)d_in[1], (const u16*)d_in[2], (const u16*)d_in[3],
                                     (const u16*)d_in[4], (const u16*)d_in[5], (const u16*)d_in[6], (const u16*)d_in[7],
                                     (const u16*)d_in[8], (const u16*)d_in[9], (const u16*)d_in[10], (const u16*)d_in[11],
                                     (const u16*)d_in[12], (const u16*)d_in[13], (const u16*)d_in[14], (const u16*)d_in[15],
                                     (float*)d_out, (char*)d_ws);
}
```

```cpp
#include <hip/hip_runtime.h>
#include <stdint.h>

typedef unsigned short u16;
typedef unsigned long long u64;
typedef unsigned v4u __attribute__((ext_vector_type(4)));
typedef unsigned v2u __attribute__((ext_vector_type(2)));
typedef float f32x4 __attribute__((ext_vector_type(4)));
typedef float f32x2 __attribute__((ext_vector_type(2)));
typedef __bf16 bf16x8 __attribute__((ext_vector_type(8)));
typedef __bf16 bf16x2 __attribute__((ext_vector_type(2)));
#define LAS __attribute__((address_space(3)))
#define GAS __attribute__((address_space(1)))
#define DEV __device__ __forceinline__

constexpr int WS_FLAG = 4096;
constexpr int WS_E = 65536;
constexpr int WS_H = 262144;
constexpr int WS_END = WS_H + 2048 * 1152 * 2;
constexpr int WS_DBG = 8 << 20;
constexpr unsigned SPIN_CAP = 400000u;
constexpr u64 TAG_STEP = 0x9E3779B97F4A7C15ull;
#define CODE_LINES 170

#define ROTATE 0
#define PREFETCH 1
#define STAMP_ON 0
#define STAMP(i) do {} while (0)
#define STAMP_FLUSH() do {} while (0)

#define WAITV(n) asm volatile("s_waitcnt vmcnt(%0)" ::"n"(n) : "memory")
#define BARRIER() asm volatile("s_waitcnt lgkmcnt(0)\n\ts_barrier" ::: "memory")
#define FENCE() asm volatile("" ::: "memory")
#define SCHED() __builtin_amdgcn_sched_barrier(0)

struct c2 { f32x2 re, im; };
DEV f32x2 fma2(f32x2 a, f32x2 b, f32x2 c) { return __builtin_elementwise_fma(a, b, c); }
DEV f32x2 sp(float a) { return f32x2{a, a}; }
DEV c2 cmul(c2 a, c2 b) { return {fma2(a.re, b.re, -(a.im * b.im)), fma2(a.re, b.im, a.im * b.re)}; }
DEV c2 cfma(c2 a, c2 b, c2 c) { return {fma2(a.re, b.re, fma2(-a.im, b.im, c.re)), fma2(a.re, b.im, fma2(a.im, b.re, c.im))}; }
DEV c2 csqr(c2 a) { return {fma2(a.re, a.re, -(a.im * a.im)), (a.re + a.re) * a.im}; }
DEV c2 el(c2 a, int t) { return {sp(a.re[t]), sp(a.im[t])}; }
DEV float bflo(unsigned v) { return __uint_as_float(v << 16); }
DEV float bfhi(unsigned v) { return __uint_as_float(v & 0xFFFF0000u); }
DEV f32x2 bf2(unsigned v) { return f32x2{bflo(v), bfhi(v)}; }
DEV unsigned pk(float lo, float hi) { f32x2 v = {lo, hi}; return __builtin_bit_cast(unsigned, __builtin_convertvector(v, bf16x2)); }
DEV unsigned pk2(f32x2 v) { return __builtin_bit_cast(unsigned, __builtin_convertvector(v, bf16x2)); }
DEV float rbf(float f) { return bflo(pk(f, f)); }
constexpr float LOG2E = 1.44269504088896341f, INV2PI = 0.159154943091895336f;
DEV f32x2 exp2v(f32x2 x) { return f32x2{__builtin_amdgcn_exp2f(x[0]), __builtin_amdgcn_exp2f(x[1])}; }
DEV c2 cpow(f32x2 l2, f32x2 rev, f32x2 n) {
  const f32x2 m = exp2v(l2 * n), t0 = rev * n;
  const float ta = __builtin_amdgcn_fractf(t0[0]), tb = __builtin_amdgcn_fractf(t0[1]);
  return {m * f32x2{__builtin_amdgcn_cosf(ta), __builtin_amdgcn_cosf(tb)}, m * f32x2{__builtin_amdgcn_sinf(ta), __builtin_amdgcn_sinf(tb)}};
}
DEV f32x2 redrev(f32x2 ai) { const f32x2 r = ai * INV2PI; return r - f32x2{__builtin_rintf(r[0]), __builtin_rintf(r[1])}; }
DEV f32x4 mfma(v4u a, v4u b, f32x4 c) {
  return __builtin_amdgcn_mfma_f32_16x16x32_bf16(__builtin_bit_cast(bf16x8, a), __builtin_bit_cast(bf16x8, b), c, 0, 0, 0);
}
DEV v4u lds128(LAS char* p) { return *(LAS v4u*)p; }
DEV v2u lds64(LAS char* p) { return *(LAS v2u*)p; }
DEV unsigned perm(unsigned hi, unsigned lo, unsigned sel) { return __builtin_amdgcn_perm(hi, lo, sel); }
DEV f32x2 swap16(f32x2 v, f32x2& hi) {
  auto a = __builtin_amdgcn_permlane16_swap(__float_as_uint(v[0]), __float_as_uint(v[0]), false, false);
  auto b = __builtin_amdgcn_permlane16_swap(__float_as_uint(v[1]), __float_as_uint(v[1]), false, false);
  hi = f32x2{__uint_as_float(a[1]), __uint_as_float(b[1])};
  return f32x2{__uint_as_float(a[0]), __uint_as_float(b[0])};
}
DEV f32x2 swap32(f32x2 v, f32x2& hi) {
  auto a = __builtin_amdgcn_permlane32_swap(__float_as_uint(v[0]), __float_as_uint(v[0]), false, false);
  auto b = __builtin_amdgcn_permlane32_swap(__float_as_uint(v[1]), __float_as_uint(v[1]), false, false);
  hi = f32x2{__uint_as_float(a[1]), __uint_as_float(b[1])};
  return f32x2{__uint_as_float(a[0]), __uint_as_float(b[0])};
}
DEV v4u quadT(unsigned x0, unsigned x1, unsigned x2, unsigned x3, bool p, bool q) {
  const unsigned a0 = __builtin_amdgcn_mov_dpp(x0, 0xB1, 0xF, 0xF, true), a1 = __builtin_amdgcn_mov_dpp(x1, 0xB1, 0xF, 0xF, true);
  const unsigned a2 = __builtin_amdgcn_mov_dpp(x2, 0xB1, 0xF, 0xF, true), a3 = __builtin_amdgcn_mov_dpp(x3, 0xB1, 0xF, 0xF, true);
  const unsigned y0 = p ? a1 : x0, y1 = p ? x1 : a0, y2 = p ? a3 : x2, y3 = p ? x3 : a2;
  const unsigned d0 = __builtin_amdgcn_mov_dpp(y0, 0x4E, 0xF, 0xF, true), d1 = __builtin_amdgcn_mov_dpp(y1, 0x4E, 0xF, 0xF, true);
  const unsigned d2 = __builtin_amdgcn_mov_dpp(y2, 0x4E, 0xF, 0xF, true), d3 = __builtin_amdgcn_mov_dpp(y3, 0x4E, 0xF, 0xF, true);
  return v4u{q ? d2 : y0, q ? d3 : y1, q ? y2 : d0, q ? y3 : d1};
}
DEV c2 cj(c2 a) { return {a.re, -a.im}; }
DEV c2 cmulj(c2 a, c2 b) { return {fma2(a.re, b.re, -(a.im * b.im)), fma2(-a.re, b.im, -(a.im * b.re))}; }
#define CAS __attribute__((address_space(4)))

__global__ __launch_bounds__(512, 2) void fused_ssm(
    const u16* __restrict__ x, const u16* __restrict__ Lre, const u16* __restrict__ Lim, const u16* __restrict__ Bre,
    const u16* __restrict__ Bim, const u16* __restrict__ Cre, const u16* __restrict__ Cim, const u16* __restrict__ ldtA,
    const u16* __restrict__ DA, const u16* __restrict__ u, const u16* __restrict__ lA, const u16* __restrict__ Aim,
    const u16* __restrict__ Cr, const u16* __restrict__ Ci, const u16* __restrict__ ldtB, const u16* __restrict__ DB,
    float* out, char* ws) {
#if defined(__HIP_DEVICE_COMPILE__)
  __shared__ __attribute__((aligned(16))) char smem_[163840];
  LAS char* smem = (LAS char*)smem_;
  u64 pc0;
  asm volatile("s_getpc_b64 %0" : "=s"(pc0));
  const int b = blockIdx.x, xcd = b & 7, j = b >> 3;
  const int tid = threadIdx.x, lane = tid & 63, w = __builtin_amdgcn_readfirstlane(tid >> 6);
  const int r = lane & 15, g = lane >> 4;
  STAMP(0);

  if (__builtin_expect(j >= 18, 0)) {
  if (j < 27) {
    const int d0 = 72 * xcd + 8 * (j - 18), d = d0 + w;
    LAS char* sc = smem + w * 20464;
    const int tb = ((tid >> 1) + 32 * (j - 18)) & 255, hf = tid & 1;
    const unsigned qa = *(const GAS unsigned*)(lA + d * 128 + 2 * lane), qi = *(const GAS unsigned*)(Aim + d * 128 + 2 * lane);
    const unsigned qr = *(const GAS unsigned*)(Cr + d * 128 + 2 * lane), qc = *(const GAS unsigned*)(Ci + d * 128 + 2 * lane);
    const v2u dk = *(const GAS v2u*)(DB + d0 + 4 * hf);
    const float dt = rbf(__builtin_amdgcn_exp2f(bflo(ldtB[d]) * LOG2E));
    const f32x2 l2 = bf2(pk2(exp2v(bf2(qa) * LOG2E))) * (-dt) * LOG2E, rv = redrev(bf2(qi) * dt);
    FENCE();
    STAMP(1);
    v2u uv[8];
    const auto rsU = __builtin_amdgcn_make_buffer_rsrc((void*)u, 0, 2048 * 1152, 0x00020000);
    const unsigned uvo = (8 * tb) * 1152 + (d0 + 4 * hf) * 2;
#define ULOAD(i) do { FENCE(); uv[i] = __builtin_amdgcn_raw_buffer_load_b64(rsU, uvo, (i) * 1152, 0); FENCE(); } while (0)
    ULOAD(0); ULOAD(1);
    f32x4 kacc[3] = {};
    {
      const c2 w1 = cpow(l2, rv, sp(1.f)), v8 = cpow(l2, -rv, sp(8.f)), Q = cpow(l2, rv, sp(128.f));
      LAS char* tw = sc + lane * 8;
      LAS char* tr = sc + r * 528 + g * 16;
#define PUTG(row, z) (*(LAS v2u*)(tw + (row) * 528) = v2u{pk(z.re[0], z.im[0]), pk(z.re[1], z.im[1])})
      *(LAS v2u*)(tw) = v2u{0x3F80u, 0x3F80u};
      c2 f = v8;
      PUTG(1, f);
#pragma unroll
      for (int i = 2; i < 16; ++i) { f = cmul(f, v8); PUTG(i, f); if (i == 4) ULOAD(2); if (i == 8) ULOAD(3); if (i == 12) ULOAD(4); }
      ULOAD(5);
      v4u fF[8];
#pragma unroll
      for (int ks = 0; ks < 8; ++ks) fF[ks] = lds128(tr + ks * 64);
      c2 R[8];
      R[0] = {bf2(qr) * (2.f * dt), bf2(qc) * (2.f * dt)};
#pragma unroll
      for (int i = 1; i < 8; ++i) R[i] = cmul(R[i - 1], w1);
      ULOAD(6);
#pragma unroll
      for (int i = 0; i < 8; ++i) { PUTG(i, R[i]); R[i] = cmul(R[i], Q); PUTG(8 + i, R[i]); if (i == 2) ULOAD(7); }
#pragma unroll
      for (int ks = 0; ks < 8; ++ks) kacc[0] = mfma(lds128(tr + ks * 64), fF[ks], kacc[0]);
#pragma unroll
      for (int cc = 0; cc < 4; ++cc) {
        const unsigned sel = (cc & 1) ? 0x07060302u : 0x05040100u;
        v4u o;
#pragma unroll
        for (int k = 0; k < 4; ++k) o[k] = perm(uv[2 * k + 1][cc >> 1], uv[2 * k][cc >> 1], sel);
        LAS char* ub = smem + (4 * hf + cc) * 20464 + 8448;
        *(LAS v4u*)(ub + 4640 + (8 * tb + 224) * 2) = o;
        LAS unsigned* u0 = (LAS unsigned*)(ub + (8 * tb + 226) * 2);
        u0[0] = o[0]; u0[1] = o[1]; u0[2] = o[2]; u0[3] = o[3];
      }
      STAMP(2);
      BARRIER();
      if (w < 4) __builtin_amdgcn_s_setprio(1);
#pragma unroll
      for (int s = 0; s < 2; ++s)
#pragma unroll
        for (int i = 0; i < 4; ++i) { R[2 * i] = cmul(R[2 * i], Q); PUTG(4 * s + i, R[2 * i]); }
#pragma unroll
      for (int s = 0; s < 4; ++s)
#pragma unroll
        for (int i = 0; i < 2; ++i) { R[4 * i] = cmul(R[4 * i], Q); PUTG(8 + 2 * s + i, R[4 * i]); }
#pragma unroll
      for (int ks = 0; ks < 8; ++ks) kacc[1] = mfma(lds128(tr + ks * 64), fF[ks], kacc[1]);
#pragma unroll
      for (int s = 0; s < 9; ++s) { R[0] = cmul(R[0], Q); PUTG(s, R[0]); }
#pragma unroll
      for (int ks = 0; ks < 8; ++ks) kacc[2] = mfma(lds128(tr + ks * 64), fF[ks], kacc[2]);
    }
    FENCE();
    STAMP(3);
    {
      LAS char* tt = sc + 17728;
      const unsigned aA = g < 2 ? (4 * r + 64 * g) * 2 : (128 + 2 * r + 64 * (g - 2)) * 2;
      *(LAS unsigned*)(tt + aA) = pk(kacc[1][0], kacc[1][1]);
      *(LAS unsigned*)(tt + aA + (g < 2 ? 4 : 64)) = pk(kacc[1][2], kacc[1][3]);
      const unsigned k01 = pk(kacc[2][0], kacc[2][1]), k23 = pk(kacc[2][2], kacc[2][3]);
      LAS char* t3 = tt + (256 + 64 * g + r) * 2;
      *(LAS u16*)(t3) = (u16)k01; *(LAS u16*)(t3 + 32) = (u16)(k01 >> 16);
      *(LAS u16*)(t3 + 64) = (u16)k23; *(LAS u16*)(t3 + 96) = (u16)(k23 >> 16);
      if (lane < 17) *(LAS unsigned*)(sc + (1023 + lane) * 4) = 0u;
      if (lane < 16) *(LAS unsigned*)(sc + 4224 + (1024 + lane) * 4) = 0u;
      if (lane < 28) { *(LAS v4u*)(sc + 8448 + lane * 16) = v4u{0, 0, 0, 0}; *(LAS v4u*)(sc + 8448 + 4640 + lane * 16) = v4u{0, 0, 0, 0}; }
      if (lane == 28) *(LAS unsigned*)(sc + 8448 + 448) = 0u;
      FENCE();
      const unsigned a1 = *(LAS unsigned*)(tt + lane * 4), b1 = *(LAS unsigned*)(tt + lane * 4 + 4);
      const unsigned a2 = *(LAS unsigned*)(tt + 256 + lane * 4), b2 = *(LAS unsigned*)(tt + 256 + lane * 4 + 4);
      const unsigned a3 = *(LAS unsigned*)(tt + 512 + lane * 4), b3 = *(LAS unsigned*)(tt + 512 + lane * 4 + 4);
      {
        const unsigned e0 = perm(a1, a1, 0x01000100u), m10 = perm(a1, a1, 0x01000302u), m21 = perm(a1, b1, 0x07060100u);
        *(LAS v2u*)(sc + 4224 + (1788 - 4 * lane) * 2) = v2u{m21, e0};
        *(LAS v2u*)(sc + (1788 - 4 * lane) * 2) = v2u{m10, e0};
      }
      {
        const unsigned e0 = perm(a2, a2, 0x01000100u), e1 = perm(a2, a2, 0x03020302u), e2 = perm(b2, b2, 0x01000100u);
        const unsigned m10 = perm(a2, a2, 0x01000302u), m21 = perm(a2, b2, 0x07060100u);
        *(LAS v4u*)(sc + 4224 + (1528 - 8 * lane) * 2) = v4u{e2, e1, m10, e0};
        *(LAS v4u*)(sc + (1528 - 8 * lane) * 2) = v4u{m21, e1, e0, e0};
      }
      {
        const unsigned e0 = perm(a3, a3, 0x01000100u), e1 = perm(a3, a3, 0x03020302u), e2 = perm(b3, b3, 0x01000100u);
        const unsigned m10 = perm(a3, a3, 0x01000302u), m21 = perm(a3, b3, 0x07060100u);
        LAS char* p1 = sc + 4224 + (1008 - 16 * lane) * 2;
        LAS char* p0_ = sc + (1008 - 16 * lane) * 2;
        *(LAS v4u*)(p1) = v4u{e2, e2, e1, e1}; *(LAS v4u*)(p1 + 16) = v4u{e1, m10, e0, e0};
        *(LAS v4u*)(p0_) = v4u{e2, m21, e1, e1}; *(LAS v4u*)(p0_ + 16) = v4u{e1, e0, e0, e0};
      }
      FENCE();
      {
        const int q0 = 8 * r + 4 * (g & 1) + 128 * (g >> 1);
        const unsigned v32 = pk(kacc[0][3], kacc[0][2]), v10 = pk(kacc[0][1], kacc[0][0]), v21 = pk(kacc[0][2], kacc[0][1]);
        *(LAS v2u*)(sc + 4224 + (2044 - q0) * 2) = v2u{v32, v10};
        *(LAS u16*)(sc + (2043 - q0) * 2) = (u16)v32;
        *(LAS unsigned*)(sc + (2044 - q0) * 2) = v21;
        *(LAS u16*)(sc + (2046 - q0) * 2) = (u16)(v10 >> 16);
      }
    }
    FENCE();
    STAMP(4);
    {
      f32x4 ya[8] = {};
      LAS char* pT = sc + (r & 1) * 4224 + 504 - 8 * (r >> 1) + 16 * g;
      LAS char* pT2 = pT + 8;
      asm volatile("" : "+v"(pT2));
      LAS char* pU = sc + 8448 + (r & 1) * 4640 + 448 + 64 * (r >> 1) + 16 * g;
      v4u U[8], Tq[2];
#define LDT(off) ({ const v2u a_ = lds64(pT + (off)), b_ = lds64(pT2 + (off)); v4u{a_[0], a_[1], b_[0], b_[1]}; })
      Tq[0] = LDT(512 * 7);
#pragma unroll
      for (int k = 7; k >= 0; --k) U[k] = lds128(pU + 512 * k);
      Tq[1] = LDT(512 * 6);
#pragma unroll 1
      for (int rr = 0; rr < 8; ++rr) {
#pragma unroll
        for (int m = 0; m < 8; ++m) {
          const v4u T = Tq[m & 1];
          Tq[m & 1] = m < 6 ? LDT(512 * (5 - m)) : LDT(512 * (13 - m) - 64);
#pragma unroll
          for (int k = 7 - m; k >= 0; --k) ya[k + m] = mfma(T, U[k], ya[k + m]);
          U[7 - m] = lds128(pU + 512 * (7 - m) - 64);
          SCHED();
        }
        pT -= 64; pT2 -= 64; pU -= 64;
      }
      FENCE();
      LAS char* py = sc + (4 * g + (r & 1) + 16 * (r >> 1)) * 4;
#pragma unroll
      for (int q = 0; q < 8; ++q) {
        *(LAS unsigned*)(py + 512 * q) = pk(ya[q][0], ya[q][1]);
        *(LAS unsigned*)(py + 512 * q + 8) = pk(ya[q][2], ya[q][3]);
      }
    }
    STAMP(5);
    BARRIER();
    {
      v4u yv[4];
#pragma unroll
      for (int cc = 0; cc < 4; ++cc) yv[cc] = lds128(smem + (4 * hf + cc) * 20464 + 16 * tb);
      float* op = out + (8 * tb) * 1152 + 576 + d0 + 4 * hf;
      const f32x2 d01 = bf2(dk[0]), d23 = bf2(dk[1]);
#pragma unroll
      for (int i = 0; i < 8; ++i) {
        const f32x2 s01 = bf2(pk2(bf2(uv[i][0]) * d01)), s23 = bf2(pk2(bf2(uv[i][1]) * d23));
        f32x2 y01, y23;
        if (i & 1) { y01 = f32x2{bfhi(yv[0][i >> 1]), bfhi(yv[1][i >> 1])}; y23 = f32x2{bfhi(yv[2][i >> 1]), bfhi(yv[3][i >> 1])}; }
        else { y01 = f32x2{bflo(yv[0][i >> 1]), bflo(yv[1][i >> 1])}; y23 = f32x2{bflo(yv[2][i >> 1]), bflo(yv[3][i >> 1])}; }
        const f32x2 o01 = bf2(pk2(y01 + s01)), o23 = bf2(pk2(y23 + s23));
        *(GAS f32x4*)(op + i * 1152) = f32x4{o01[0], o01[1], o23[0], o23[1]};
      }
    }
    STAMP(6);
    STAMP(15);
    STAMP_FLUSH();
  } else if (b != 255) {
    unsigned v = 0;
    if (j == 27 && tid < CODE_LINES) v = *(const GAS unsigned*)((pc0 & ~127ull) + tid * 128);
    if (j < 31) {
      const int R = ((j - 27) * 8 + w) * 36 + (lane < 36 ? lane : 35);
      const u64 bx = (u64)x + (xcd >> 1) * 512 * 1152, br = (u64)Bre + ((xcd & 1) * 256 - 512) * 1152, bi = (u64)Bim + ((xcd & 1) * 256 - 832) * 1152;
      const u64 base = (R < 512 ? bx : (R < 832 ? br : bi)) + R * 1152;
      unsigned t[9];
#pragma unroll
      for (int k = 0; k < 9; ++k) t[k] = *(const GAS unsigned*)(base + k * 128);
#pragma unroll
      for (int k = 0; k < 9; ++k) v ^= t[k];
    }
    asm volatile("" ::"v"(v));
  } else if (w == 0) {
    const u64 tag = *(const CAS u64*)ws + TAG_STEP;
    const GAS u64* fl = (const GAS u64*)(ws + WS_FLAG);
#pragma unroll 1
    for (unsigned sp_ = 0; sp_ < SPIN_CAP; ++sp_) {
      __builtin_amdgcn_s_sleep(16);
      FENCE();
      const u64 f0 = __hip_atomic_load(fl + lane * 16, __ATOMIC_RELAXED, __HIP_MEMORY_SCOPE_AGENT);
      const u64 f1 = __hip_atomic_load(fl + (64 + lane) * 16, __ATOMIC_RELAXED, __HIP_MEMORY_SCOPE_AGENT);
      const u64 f2 = __hip_atomic_load(fl + (128 + (lane & 15)) * 16, __ATOMIC_RELAXED, __HIP_MEMORY_SCOPE_AGENT);
      if (__all(f0 == tag && f1 == tag && f2 == tag)) break;
    }
    if (lane == 0) __hip_atomic_store((GAS u64*)ws, tag, __ATOMIC_RELAXED, __HIP_MEMORY_SCOPE_AGENT);
  }
  return;
  }
  {
    const int odd = xcd & 1, mid = j >> 4;
    const int pi = mid ? (j - 16) + 2 * odd : (j >> 2), gi = mid ? 4 : (j & 3);
    const int pg = mid ? 4 : gi + 5 * odd, c = 4 * (xcd >> 1) + pi;
    const int wm = w & 3, wn = w >> 2;
    const int p0 = 64 * pg + 32 * wn + 2 * r;
    const unsigned pLr = *(const GAS unsigned*)(Lre + p0), pLi = *(const GAS unsigned*)(Lim + p0), pDt = *(const GAS unsigned*)(ldtA + p0);
    FENCE();
    const u64 sx = (u64)x + (c * 128 + w * 32) * 1152, sb = (u64)(w < 6 ? Bre : Bim) + (pg * 64 + (w & 1) * 32) * 1152;
    const auto rsS = __builtin_amdgcn_make_buffer_rsrc((void*)(wn ? sb : sx), 0, 32 * 1152, 0x00020000);
    const int l3 = lane >> 3, ch = (lane & 7) ^ (l3 >> 1);
    const int rowx = 8 * (l3 >> 2) + 4 * (l3 & 1) + ((l3 >> 1) & 1), rowb = rowx ^ ((rowx ^ (2 * l3)) & -wn);
    const unsigned dvA = __umul24(rowb, 1152u) + ch * 16, dvB = dvA + 16 * 1152 + 64 - 2 * (dvA & 64);
    const int stp = (2 - wn) * 1152;
#define SLOT(s) (((8 - (s)) % 3) * 32768)
#define SPIECE(s, i) __builtin_amdgcn_raw_ptr_buffer_load_lds(rsS, smem + SLOT(s) + w * 4096 + (i) * 1024, 16, ((i) & 1) ? dvB : dvA, (s) * 128 + ((i) >> 1) * stp, 0, 0)
    SPIECE(0, 0); SPIECE(0, 1); SPIECE(0, 2); SPIECE(0, 3);
    asm volatile("s_barrier" ::: "memory");
    SPIECE(1, 0); SPIECE(1, 1); SPIECE(1, 2); SPIECE(1, 3);
    FENCE();
    const u64 tag = *(const CAS u64*)ws + TAG_STEP;
    const unsigned tagA = (unsigned)tag, tagB = (unsigned)(tag >> 32);
    const auto rsW = __builtin_amdgcn_make_buffer_rsrc(ws, 0, WS_END, 0x00020000);
    const int c2_ = 2 * xcd + (j >= 9), n = j >= 9 ? j - 9 : j;
    const auto rsCre = __builtin_amdgcn_make_buffer_rsrc((void*)(Cre + 64 * n * 576), 0, 64 * 1152, 0x00020000);
    const auto rsCim = __builtin_amdgcn_make_buffer_rsrc((void*)(Cim + 64 * n * 576), 0, 64 * 1152, 0x00020000);
    const unsigned cvo = __umul24(32 * (w & 1) + 4 * l3 + (w >> 1), 1152u) + (((lane & 7) ^ ((4 * w + (l3 >> 1)) & 7)) * 16);
#define CPIECE(q) __builtin_amdgcn_raw_ptr_buffer_load_lds((q) < 9 ? rsCre : rsCim, smem + 16384 + (q) * 8192 + w * 1024, 16, cvo, ((q) % 9) * 128, 0, 0)

    const f32x2 lr = bf2(pLr), li = bf2(pLi), dtA = bf2(pk2(exp2v(bf2(pDt) * LOG2E)));
    c2 lam = cpow(dtA * lr * LOG2E, redrev(dtA * li), sp(1.f));
    c2 l4 = csqr(csqr(lam)), l8 = csqr(l4), l16 = csqr(l8), l32 = csqr(l16);
    asm volatile("" : "+v"(lam.re), "+v"(lam.im), "+v"(l4.re), "+v"(l4.im), "+v"(l8.re), "+v"(l8.im), "+v"(l16.re), "+v"(l16.im), "+v"(l32.re), "+v"(l32.im));

    const unsigned fo = r * 128 + ((g ^ (r >> 1)) * 16);
    LAS char* pa0 = smem + wm * 4096 + fo;
    LAS char* pa1 = smem + wm * 4096 + (fo ^ 64);
    LAS char* pb0 = smem + 16384 + wn * 4096 + fo;
    LAS char* pb1 = smem + 16384 + wn * 4096 + (fo ^ 64);
    f32x4 acc[2][2][2] = {};
    v4u fr[2][12];
    STAMP(1);
#pragma unroll
    for (int s = 0; s < 10; ++s) {
      if (s <= 6) WAITV(4); else if (s <= 8) WAITV(8);
      if (s == 1) asm volatile("" ::"s"(tagA), "s"(tagB));
      BARRIER();
      if (s == 0) STAMP(2);
      if (s == 3) STAMP(11);
      if (s == 6) STAMP(12);
      if (s == 9) STAMP(13);
#pragma unroll
      for (int i = 0; i < 16; ++i) {
        if (s < 9 && i < 12) {
          const int kk = i / 6, e = i % 6;
          LAS char* pa = (kk ? pa1 : pa0) + SLOT(s);
          LAS char* pb = (kk ? pb1 : pb0) + SLOT(s);
          fr[s & 1][i] = e == 0 ? lds128(pa) : e == 5 ? lds128(pa + 2048) : lds128(pb + ((e - 1) >> 1) * 8192 + ((e - 1) & 1) * 2048);
        }
        if (s >= 1) {
          const int kk = i >> 3, m = (i >> 2) & 1, e = i & 3;
          acc[m][e >> 1][e & 1] = mfma(fr[(s - 1) & 1][6 * kk + 5 * m], fr[(s - 1) & 1][6 * kk + 1 + e], acc[m][e >> 1][e & 1]);
        }
        if ((i & 3) == 3) {
          if (s + 2 < 9) SPIECE(s + 2, i >> 2);
          if (s == 7) CPIECE(14 + (i >> 2));
          if (s == 8) CPIECE(6 + (i >> 2));
        }
        SCHED();
      }
      if (s == 6) { CPIECE(10); CPIECE(11); CPIECE(12); CPIECE(13); SCHED(); }
    }
    STAMP(3);
#define BSTEP(t, s) c2{f32x2{acc[(s) >> 1][0][t][2 * ((s) & 1)], acc[(s) >> 1][0][t][2 * ((s) & 1) + 1]}, f32x2{acc[(s) >> 1][1][t][2 * ((s) & 1)], acc[(s) >> 1][1][t][2 * ((s) & 1) + 1]}}
    c2 eA, ea, ea2;
    LAS char* sW = smem;
    LAS char* sH = smem + 2048;
    {
      c2 e2[2];
#pragma unroll
      for (int t = 0; t < 2; ++t) {
        e2[t] = BSTEP(t, 0);
#pragma unroll
        for (int s = 1; s < 4; ++s) e2[t] = cfma(el(lam, t), e2[t], BSTEP(t, s));
      }
      eA = {f32x2{e2[0].re[0], e2[1].re[0]}, f32x2{e2[0].im[0], e2[1].im[0]}};
      const c2 e = cfma(l4, eA, c2{f32x2{e2[0].re[1], e2[1].re[1]}, f32x2{e2[0].im[1], e2[1].im[1]}});
      c2 hi;
      ea.re = swap16(e.re, hi.re); ea.im = swap16(e.im, hi.im);
      const c2 A = cfma(l8, ea, hi);
      ea2.re = swap32(A.re, hi.re); ea2.im = swap32(A.im, hi.im);
      const c2 W = cfma(l16, ea2, hi);
      if (g == 0) *(LAS f32x4*)(sW + (wm * 32 + 16 * wn + r) * 16) = f32x4{W.re[0], W.re[1], W.im[0], W.im[1]};
    }
    const bool pub = w == 0 || w == 5;
    if (pub) __builtin_amdgcn_s_setprio(2);
    BARRIER();
    c2 pw;
    {
      f32x4 wv[4];
#pragma unroll
      for (int i = 0; i < 4; ++i) wv[i] = *(LAS f32x4*)(sW + (i * 32 + 16 * wn + r) * 16);
      const c2 s1 = {wv[0].xy, wv[0].zw};
      const c2 s2 = cfma(l32, s1, c2{wv[1].xy, wv[1].zw});
      const c2 s3 = cfma(l32, s2, c2{wv[2].xy, wv[2].zw});
      if (pub) {
        const c2 E = cfma(l32, s3, c2{wv[3].xy, wv[3].zw});
        if (g < 2)
          __builtin_amdgcn_raw_buffer_store_b128(v4u{__float_as_uint(g ? E.re[1] : E.re[0]), tagA, __float_as_uint(g ? E.im[1] : E.im[0]), tagB}, rsW,
                                                 WS_E + (c * 576 + p0 + g) * 16, 0, 16);
        __builtin_amdgcn_s_setprio(0);
      }
      pw.re = wm == 0 ? sp(0.f) : wm == 1 ? s1.re : wm == 2 ? s2.re : s3.re;
      pw.im = wm == 0 ? sp(0.f) : wm == 1 ? s1.im : wm == 2 ? s2.im : s3.im;
    }
    FENCE();
    STAMP(4);
    const unsigned qLr = Lre[64 * pg + lane], qLi = Lim[64 * pg + lane], qDt = ldtA[64 * pg + lane];
    FENCE();
    CPIECE(0); CPIECE(1); CPIECE(2); CPIECE(3); CPIECE(4); CPIECE(5);
    FENCE();
    const int t2 = 128 * c2_ + 32 * (w >> 1) + 4 * (w & 1) + 8 * g;
    v2u xs[4];
#pragma unroll
    for (int i = 0; i < 4; ++i) xs[i] = *(const GAS v2u*)(x + (t2 + i) * 576 + 64 * n + 4 * r);
    const v2u dsk = *(const GAS v2u*)(DA + 64 * n + 4 * r);
    FENCE();
    c2 pf2[2], lp2[2], bs[2][4];
    {
      const f32x2 den = lr * lr + li * li, inv = f32x2{__builtin_amdgcn_rcpf(den[0]), __builtin_amdgcn_rcpf(den[1])} * 2.f, nr = lam.re - 1.f;
      const c2 coef = {(nr * lr + lam.im * li) * inv, (lam.im * lr - nr * li) * inv};
      const c2 z = {sp(0.f), sp(0.f)}, one = {sp(1.f), sp(0.f)};
      const c2 lg = cmul((g & 1) ? l8 : one, (g & 2) ? l16 : one);
      const c2 lwv = cmul((wm & 1) ? l32 : one, (wm & 2) ? csqr(l32) : one);
      const c2 lw = cmul(cmul(coef, lg), lwv);
      const c2 o = (g & 2) ? ea2 : z;
      const c2 pin = (g & 1) ? cfma(l8, o, ea) : o;
      const c2 pfA = cmul(coef, cfma(lg, pw, pin));
      const c2 pfB = cfma(l4, pfA, cmul(coef, eA)), lwB = cmul(l4, lw);
#pragma unroll
      for (int t = 0; t < 2; ++t) {
        pf2[t] = {f32x2{pfA.re[t], pfB.re[t]}, f32x2{-pfA.im[t], -pfB.im[t]}};
        lp2[t] = {f32x2{lw.re[t], lwB.re[t]}, f32x2{-lw.im[t], -lwB.im[t]}};
#pragma unroll
        for (int s = 0; s < 4; ++s) {
          bs[t][s] = cmulj(el(coef, t), BSTEP(t, s));
          asm volatile("" : "+v"(bs[t][s].re), "+v"(bs[t][s].im));
        }
        asm volatile("" : "+v"(pf2[t].re), "+v"(pf2[t].im), "+v"(lp2[t].re), "+v"(lp2[t].im));
      }
    }
    if (w < 4) {
      float hr = 0.f, hi = 0.f;
      if (w < c) {
        const float dt = rbf(__builtin_amdgcn_exp2f(bflo(qDt) * LOG2E));
        const f32x2 ql2 = sp(dt * bflo(qLr) * LOG2E), qrv = redrev(sp(dt * bflo(qLi)));
        const int n0 = c - 1 - w;
        const c2 wa = cpow(ql2, qrv, f32x2{(float)(128 * n0), (float)(128 * (n0 > 4 ? n0 - 4 : 0))});
        const c2 wb = cpow(ql2, qrv, f32x2{(float)(128 * (n0 > 8 ? n0 - 8 : 0)), (float)(128 * (n0 > 12 ? n0 - 12 : 0))});
        const unsigned eo = WS_E + (w * 576 + 64 * pg + lane) * 16;
        const unsigned o1 = n0 >= 4 ? 4 * 9216 : 0, o2 = n0 >= 8 ? 8 * 9216 : 0, o3 = n0 >= 12 ? 12 * 9216 : 0;
        const f32x2 ma = {1.f, n0 >= 4 ? 1.f : 0.f}, mb = {n0 >= 8 ? 1.f : 0.f, n0 >= 12 ? 1.f : 0.f};
        c2 wa2 = {wa.re * ma, wa.im * ma}, wb2 = {wb.re * mb, wb.im * mb};
        asm volatile("" : "+v"(wa2.re), "+v"(wa2.im), "+v"(wb2.re), "+v"(wb2.im));
        v4u e0, e1, e2, e3;
#pragma unroll 1
        for (unsigned sp_ = 0; sp_ < SPIN_CAP; ++sp_) {
          FENCE();
          e0 = __builtin_amdgcn_raw_buffer_load_b128(rsW, eo, 0, 16);
          e1 = __builtin_amdgcn_raw_buffer_load_b128(rsW, eo, o1, 16);
          e2 = __builtin_amdgcn_raw_buffer_load_b128(rsW, eo, o2, 16);
          e3 = __builtin_amdgcn_raw_buffer_load_b128(rsW, eo, o3, 16);
          const unsigned bad = ((e0[1] ^ tagA) | (e0[3] ^ tagB) | (e1[1] ^ tagA)) | ((e1[3] ^ tagB) | (e2[1] ^ tagA) | (e2[3] ^ tagB)) | ((e3[1] ^ tagA) | (e3[3] ^ tagB));
          if (__all(bad == 0)) break;
          __builtin_amdgcn_s_sleep(1);
        }
        const c2 hp = cfma(wa2, c2{f32x2{__uint_as_float(e0[0]), __uint_as_float(e1[0])}, f32x2{__uint_as_float(e0[2]), __uint_as_float(e1[2])}},
                           cmul(wb2, c2{f32x2{__uint_as_float(e2[0]), __uint_as_float(e3[0])}, f32x2{__uint_as_float(e2[2]), __uint_as_float(e3[2])}}));
        hr = hp.re[0] + hp.re[1]; hi = hp.im[0] + hp.im[1];
      }
      LAS float* hq = (LAS float*)(sH + (w * 32 + (lane >> 1)) * 16) + (lane & 1);
      hq[0] = hr; hq[2] = -hi;
    }
    STAMP(5);
    BARRIER();
    {
      const f32x4 hs = (*(LAS f32x4*)(sH + (16 * wn + r) * 16) + *(LAS f32x4*)(sH + (32 + 16 * wn + r) * 16)) +
                       (*(LAS f32x4*)(sH + (64 + 16 * wn + r) * 16) + *(LAS f32x4*)(sH + (96 + 16 * wn + r) * 16));
      const c2 H = {hs.xy, hs.zw}, lamj = cj(lam);
      c2 h0 = cfma(lp2[0], el(H, 0), pf2[0]), h1 = cfma(lp2[1], el(H, 1), pf2[1]);
      unsigned dv[4][4];
#pragma unroll
      for (int s = 0; s < 4; ++s) {
        h0 = cfma(el(lamj, 0), h0, bs[0][s]);
        h1 = cfma(el(lamj, 1), h1, bs[1][s]);
        dv[0][s] = pk(h0.re[0], h1.re[0]); dv[2][s] = pk(h0.re[1], h1.re[1]);
        dv[1][s] = pk(h0.im[0], h1.im[0]); dv[3][s] = pk(h0.im[1], h1.im[1]);
      }
      LAS char* tb_ = smem + 8192 + w * 1024;
      LAS char* tw_ = tb_ + ((r >> 2) * 16 + 4 * g) * 16 + (r & 3) * 4;
      LAS char* tr_ = tb_ + (lane ^ g) * 16;
      const unsigned hoff = WS_H + (4 * c + wm) * 2 * 36864 + (2 * pg + wn) * 2048 + lane * 16;
#pragma unroll
      for (int e = 0; e < 4; ++e) {
#pragma unroll
        for (int s = 0; s < 4; ++s) *(LAS unsigned*)(tw_ + ((s ^ (r >> 2)) * 16)) = dv[e][s];
        FENCE();
        const v4u o = lds128(tr_);
        FENCE();
        __builtin_amdgcn_raw_buffer_store_b128(o, rsW, hoff + (e >> 1) * 36864 + (e & 1) * 1024, 0, 16);
      }
    }
    STAMP(6);
    WAITV(0);
    BARRIER();
    STAMP(7);
    if (w == 0) {
      if (lane == 0) __hip_atomic_store((GAS u64*)(ws + WS_FLAG) + (c * 9 + pg) * 16, tag, __ATOMIC_RELAXED, __HIP_MEMORY_SCOPE_AGENT);
      const GAS u64* fp = (const GAS u64*)(ws + WS_FLAG) + (c2_ * 9 + (lane < 9 ? lane : 0)) * 16;
#pragma unroll 1
      for (unsigned sp_ = 0; sp_ < SPIN_CAP; ++sp_) {
        FENCE();
        if (__all(__hip_atomic_load(fp, __ATOMIC_RELAXED, __HIP_MEMORY_SCOPE_AGENT) == tag)) break;
        __builtin_amdgcn_s_sleep(1);
      }
    }
    BARRIER();
    STAMP(8);
    {
      constexpr int HD = 8;
      const unsigned hsrc = WS_H + (c2_ * 8 + w) * 36864;
      v4u hf[HD];
#pragma unroll
      for (int i = 0; i < HD; ++i) hf[i] = __builtin_amdgcn_raw_buffer_load_b128(rsW, lane * 16, hsrc + i * 1024, 16);
      LAS char* pc0_ = smem + 16384 + fo;
      LAS char* pc1_ = smem + 16384 + (fo ^ 64);
      f32x4 a[4] = {};
      v4u cf[2][4];
#define CFRAG(k1, jt) lds128(((((k1) >> 1) & 1) ? pc1_ : pc0_) + (((k1) & 1) * 9 + ((k1) >> 2)) * 8192 + (jt) * 2048)
#pragma unroll
      for (int jt = 0; jt < 4; ++jt) cf[0][jt] = CFRAG(0, jt);
#pragma unroll
      for (int ks = 0; ks < 36; ++ks) {
        const v4u hc = hf[ks % HD];
#pragma unroll
        for (int jt = 0; jt < 4; ++jt) {
          if (ks + 1 < 36) cf[(ks + 1) & 1][jt] = CFRAG(ks + 1, jt);
          a[jt] = mfma(hc, cf[ks & 1][jt], a[jt]);
          SCHED();
        }
        if (ks + HD < 36) hf[ks % HD] = __builtin_amdgcn_raw_buffer_load_b128(rsW, lane * 16, hsrc + (ks + HD) * 1024, 16);
        SCHED();
        if (ks == 0) STAMP(9);
      }
      STAMP(10);
      const f32x2 d01 = bf2(dsk[0]), d23 = bf2(dsk[1]);
#pragma unroll
      for (int i = 0; i < 4; ++i) {
        const f32x2 s01 = bf2(pk2(bf2(xs[i][0]) * d01)), s23 = bf2(pk2(bf2(xs[i][1]) * d23));
        *(GAS f32x4*)(out + (t2 + i) * 1152 + 64 * n + 4 * r) = f32x4{a[0][i] + s01[0], a[1][i] + s01[1], a[2][i] + s23[0], a[3][i] + s23[1]};
      }
    }
    STAMP(15);
    STAMP_FLUSH();
  }
#endif
}

extern "C" void kernel_launch(void* const* d_in, const int* in_sizes, int n_in, void* d_out, int out_size, void* d_ws,
                              size_t ws_size, hipStream_t stream) {
  fused_ssm<<<256, 512, 0, stream>>>((const u16*)d_in[0], (const u16*)d_in[1], (const u16*)d_in[2], (const u16*)d_in[3],
                                     (const u16*)d_in[4], (const u16*)d_in[5], (const u16*)d_in[6], (const u16*)d_in[7],
                                     (const u16*)d_in[8], (const u16*)d_in[9], (const u16*)d_in[10], (const u16*)d_in[11],
                                     (const u16*)d_in[12], (const u16*)d_in[13], (const u16*)d_in[14], (const u16*)d_in[15],
                                     (float*)d_out, (char*)d_ws);
}
```
